```python
import torch

def _softmax(x, axis=-1):
    return torch.softmax(x, dim=axis)

def _gelu(x):
    return torch.nn.functional.gelu(x, approximate='tanh')

def _silu(x):
    return torch.nn.functional.silu(x)

def _sigmoid(x):
    return torch.sigmoid(x)

def _layernorm(x, axis=-1, eps=1e-05):
    m = x.mean(dim=axis, keepdim=True)
    v = x.var(dim=axis, keepdim=True, unbiased=False)
    return (x - m) / torch.sqrt(v + eps)

def _scatter_add(tgt, idx, src, axis=0):
    return tgt.clone().index_add_(axis, idx, src)

def _segment_sum(src, seg, n):
    out = torch.zeros((n,) + src.shape[1:], dtype=src.dtype)
    return out.index_add_(0, seg, src)

def _pad(x, pw):
    flat = tuple((v for pair in reversed(pw) for v in pair))
    return torch.nn.functional.pad(x, flat)
kernel_name = 'gated_mix__linear_kernel_hedgehog__memory_aug_recurrent_mem_tr__prompt'

def setup_inputs():

    _w = lambda a: (lambda t: t.to(torch.bfloat16) if t.is_floating_point() else t)(torch.as_tensor(a))

    def _A_setup():
        B, S, D, H = (1, 2048, 448, 16)
        Dh = D // H
        return {'x': _w((lambda *s: torch.randn(*s, dtype=torch.bfloat16) * 0.05)(B, S, D)), 'wqkv': _w((lambda *s: torch.randn(*s, dtype=torch.bfloat16) * 1.6)(D, 3 * D)), 'w_phi': _w((lambda *s: torch.randn(*s, dtype=torch.bfloat16) * 1.6)(H, Dh, Dh) * Dh ** (-0.5)), 'wo': _w((lambda *s: torch.randn(*s, dtype=torch.bfloat16) * 0.05)(D, D))}
    _A_in = _A_setup()

    def _B_setup():
        B, S, Dm = (1, 2048, 448)
        H, Dh = (16, 256)
        M, seg = (8, 32)
        seg = min(seg, S)
        S = S // seg * seg
        return {'x': _w((lambda *s: torch.randn(*s, dtype=torch.bfloat16) * 0.05)(B, S, Dm)), 'mem0': _w((lambda *s: torch.randn(*s, dtype=torch.bfloat16) * 0.8)(B, M, Dm)), 'Wq': _w((lambda *s: torch.randn(*s, dtype=torch.bfloat16) * 1.6)(Dm, H, Dh) * Dm ** (-0.5)), 'Wk': _w((lambda *s: torch.randn(*s, dtype=torch.bfloat16) * 1.6)(Dm, H, Dh) * Dm ** (-0.5)), 'Wv': _w((lambda *s: torch.randn(*s, dtype=torch.bfloat16) * 0.8)(Dm, H, Dh) * Dm ** (-0.5)), 'Wo': _w((lambda *s: torch.randn(*s, dtype=torch.bfloat16) * 0.8)(H, Dh, Dm) * (H * Dh) ** (-0.5)), 'seg_len': seg}
    _B_in = _B_setup()
    return {'A_x': _A_in['x'], 'A_wqkv': _A_in['wqkv'], 'A_w_phi': _A_in['w_phi'], 'A_wo': _A_in['wo'], 'B_x': _B_in['x'], 'B_mem0': _B_in['mem0'], 'B_Wq': _B_in['Wq'], 'B_Wk': _B_in['Wk'], 'B_Wv': _B_in['Wv'], 'B_Wo': _B_in['Wo'], 'B_seg_len': _B_in['seg_len'], 'gate_w': _w((lambda *s: torch.randn(*s, dtype=torch.bfloat16) * 0.05)(448) * 16.0)}

def reference(A_x, A_wqkv, A_w_phi, A_wo, B_x, B_mem0, B_Wq, B_Wk, B_Wv, B_Wo, B_seg_len, gate_w):

    def _A_ref(x, wqkv, w_phi, wo):
        B, S, D = x.shape
        H = 16
        Dh = D // H
        qkv = (lambda x, *d: x.permute(*d))((x @ wqkv).reshape(B, S, 3, H, Dh), 2, 0, 3, 1, 4)
        q, k, v = (qkv[0], qkv[1], qkv[2])
        pq = torch.einsum('bhsd,hdf->bhsf', q, w_phi)
        pk = torch.einsum('bhsd,hdf->bhsf', k, w_phi)
        phi_q = torch.concatenate([_softmax(pq, axis=-1), _softmax(-pq, axis=-1)], axis=-1)
        phi_k = torch.concatenate([_softmax(pk, axis=-1), _softmax(-pk, axis=-1)], axis=-1)
        kv = torch.einsum('bhsf,bhsd->bhfd', phi_k, v)
        z = torch.einsum('bhsf->bhf', phi_k)
        num = torch.einsum('bhqf,bhfd->bhqd', phi_q, kv)
        den = torch.einsum('bhqf,bhf->bhq', phi_q, z)[..., None] + 1e-06
        o = num / den
        return (lambda x, *d: x.permute(*d))(o, 0, 2, 1, 3).reshape(B, S, D) @ wo

    def _B_ref(x, mem0, Wq, Wk, Wv, Wo, seg_len):
        B, S, Dm = x.shape
        H, Dh = (Wq.shape[1], Wq.shape[2])
        M = mem0.shape[1]
        scale = Dh ** (-0.5)
        n_seg = S // seg_len
        L = M + seg_len
        idx = torch.arange(L)
        mask = (idx[:, None] < M) | (idx[:, None] >= idx[None, :])
        mem = mem0
        outs = []
        for s in range(n_seg):
            xs = x[:, s * seg_len:(s + 1) * seg_len, :].clone()
            h = torch.concatenate([mem, xs], axis=1)
            q = torch.einsum('btd,dhe->bhte', h, Wq)
            k = torch.einsum('btd,dhe->bhte', h, Wk)
            v = torch.einsum('btd,dhe->bhte', h, Wv)
            scores = torch.einsum('bhqe,bhke->bhqk', q, k) * scale
            scores = torch.where(mask[None, None], scores, -1000000000.0)
            p = _softmax(scores, axis=-1)
            o = torch.einsum('bhqk,bhke->bhqe', p, v)
            o = torch.einsum('bhte,hed->btd', o, Wo)
            mem = o[:, :M, :].clone()
            outs.append(o[:, M:, :])
        return torch.concatenate(outs, axis=1)
    x = A_x
    g = _sigmoid((x * gate_w).sum(-1, keepdims=True))
    a = _A_ref(x, A_wqkv, A_w_phi, A_wo)
    b = _B_ref(x, B_mem0, B_Wq, B_Wk, B_Wv, B_Wo, B_seg_len)
    return g * a + (1 - g) * b
```

```cpp
#include <hip/hip_runtime.h>
#include <hip/hip_bf16.h>

#define LAS __attribute__((address_space(3)))
#define GAS __attribute__((address_space(1)))
#define DI __device__ __forceinline__
typedef unsigned short u16;
typedef __bf16 bf16x8 __attribute__((ext_vector_type(8)));
typedef __bf16 bf16x16 __attribute__((ext_vector_type(16)));
typedef __bf16 bf16x4 __attribute__((ext_vector_type(4)));
typedef __bf16 bf16x2 __attribute__((ext_vector_type(2)));
typedef float f32x4 __attribute__((ext_vector_type(4)));
typedef float f32x2 __attribute__((ext_vector_type(2)));
typedef float f32x16 __attribute__((ext_vector_type(16)));
typedef unsigned u32x4 __attribute__((ext_vector_type(4)));
typedef unsigned u32x2 __attribute__((ext_vector_type(2)));
typedef unsigned u32x8 __attribute__((ext_vector_type(8)));

constexpr int S = 2048, D = 448, H = 16, NSEG = 64;
constexpr unsigned SENT = 0xFFFFFFFFu;
constexpr float LOG2E = 1.4426950408889634f;

constexpr unsigned BLK = 16384, STEPB = 4 * BLK;
constexpr unsigned OFF_MEM = 0, OFF_SC = BLK, OFF_ML = BLK + 4096, OFF_ET = 2 * BLK, OFF_CAL = 3 * BLK, OFF_FLG = 3 * BLK + 1024;
constexpr size_t MB = 1u << 20;
constexpr size_t WS_HAND = 0;
constexpr size_t WS_WAF = 4 * MB, WS_WBF = 11 * MB;
constexpr size_t WS_WQF = 18 * MB, WS_WKF = WS_WQF + MB / 2, WS_WVF = WS_WQF + MB, WS_WOF = WS_WQF + 3 * MB / 2;
constexpr size_t WS_UT = 20 * MB, WS_VT = 49 * MB;
constexpr size_t WS_ETT = 78 * MB, WS_MLT = 80 * MB;
constexpr size_t WS_UM = 81 * MB, WS_VM = 90 * MB;
constexpr size_t WS_PQ = 98 * MB, WS_PK = 100 * MB, WS_V = 102 * MB;
constexpr size_t WS_KVP = 104 * MB, WS_KVF = 105 * MB;
constexpr size_t WS_GA = 106 * MB;
constexpr size_t WS_G = 110 * MB;
constexpr size_t WS_XF = 111 * MB;
constexpr size_t WS_XP = 116 * MB;
constexpr size_t WS_CNT = 114 * MB;
constexpr size_t WS_BLK = WS_CNT + 65536;
constexpr size_t WS_DBG = 118 * MB;
constexpr size_t WS_END = 128 * MB;
constexpr int FR = 14336;
constexpr int C_TILE = 0, C_CHAIN = 16, C_PQ = 80, C_PK = 96, C_V = 112, C_KVT = 128, C_KV = 144, C_GA = 160, C_PAR = 176, C_AUXW = 177, C_AUXO = 178, C_AUXX = 179, C_ETT = 192, C_N = 208;
constexpr unsigned TILE_DONE = 160;

#define CHAIN_ASM_TEXT \
    "s_load_dwordx4 s[36:39], %0, 0x0\n" \
    "s_load_dwordx4 s[44:47], %0, 0x10\n" \
    "s_load_dwordx4 s[48:51], %0, 0x20\n" \
    "s_load_dwordx4 s[52:55], %0, 0x30\n" \
    "s_load_dwordx4 s[88:91], %0, 0x40\n" \
    "s_load_dwordx4 s[92:95], %0, 0x50\n" \
    "s_load_dwordx2 s[56:57], %0, 0x60\n" \
    "s_load_dwordx2 s[40:41], %0, 0x68\n" \
    "s_load_dwordx2 s[98:99], %0, 0x70\n" \
    "s_load_dwordx8 s[60:67], %0, 0x80\n" \
    "s_load_dwordx4 s[68:71], %0, 0xa0\n" \
    "s_load_dword s72, %0, 0xb0\n" \
    "s_load_dwordx2 s[74:75], %0, 0xb8\n" \
    "s_load_dwordx2 s[82:83], %0, 0xc0\n" \
    "s_load_dwordx2 s[84:85], %0, 0xc8\n" \
    "s_load_dword s86, %0, 0xd0\n" \
    "v_mbcnt_lo_u32_b32 v248, -1, 0\n" \
    "v_mbcnt_hi_u32_b32 v248, -1, v248\n" \
    "v_lshlrev_b32 v248, 2, v248\n" \
    "s_waitcnt lgkmcnt(0)\n" \
    "s_not_b64 s[58:59], s[74:75]\n" \
    "s_lshl_b32 s67, %1, 12\n" \
    "s_add_u32 s63, s63, s67\n" \
    "s_lshl_b32 s67, %1, 10\n" \
    "s_add_u32 s65, s65, s67\n" \
    "s_cmp_eq_u32 %1, 1\n" \
    "s_cbranch_scc1 LwW1_%=\n" \
    "s_cmp_eq_u32 %1, 2\n" \
    "s_cbranch_scc1 LwW2_%=\n" \
    "s_cmp_eq_u32 %1, 3\n" \
    "s_cbranch_scc1 LwW3_%=\n" \
    "LwW0_%=:\n" \
    "v_add_u32 v248, 0, v248\n" \
    "ds_read_b32 v0, v248\n" \
    "ds_read_b32 v1, v248 offset:256\n" \
    "ds_read_b32 v2, v248 offset:512\n" \
    "ds_read_b32 v3, v248 offset:768\n" \
    "ds_read_b32 v4, v248 offset:1024\n" \
    "ds_read_b32 v5, v248 offset:1280\n" \
    "ds_read_b32 v6, v248 offset:1536\n" \
    "ds_read_b32 v7, v248 offset:1792\n" \
    "s_waitcnt lgkmcnt(0)\n" \
    "ds_read_b32 v8, v248 offset:2048\n" \
    "ds_read_b32 v9, v248 offset:2304\n" \
    "ds_read_b32 v10, v248 offset:2560\n" \
    "ds_read_b32 v11, v248 offset:2816\n" \
    "ds_read_b32 v12, v248 offset:3072\n" \
    "ds_read_b32 v13, v248 offset:3328\n" \
    "ds_read_b32 v14, v248 offset:3584\n" \
    "ds_read_b32 v15, v248 offset:3840\n" \
    "s_waitcnt lgkmcnt(0)\n" \
    "ds_read_b32 v16, v248 offset:4096\n" \
    "ds_read_b32 v17, v248 offset:4352\n" \
    "ds_read_b32 v18, v248 offset:4608\n" \
    "ds_read_b32 v19, v248 offset:4864\n" \
    "ds_read_b32 v20, v248 offset:5120\n" \
    "ds_read_b32 v21, v248 offset:5376\n" \
    "ds_read_b32 v22, v248 offset:5632\n" \
    "ds_read_b32 v23, v248 offset:5888\n" \
    "s_waitcnt lgkmcnt(0)\n" \
    "ds_read_b32 v24, v248 offset:6144\n" \
    "s_waitcnt lgkmcnt(0)\n" \
    "s_add_u32 s67, s85, 0\n" \
    "buffer_load_dwordx4 a[0:3], v20, s[88:91], s67 offen\n" \
    "s_add_u32 s67, s85, 28672\n" \
    "buffer_load_dwordx4 a[4:7], v20, s[88:91], s67 offen\n" \
    "s_add_u32 s67, s85, 229376\n" \
    "buffer_load_dwordx4 a[8:11], v20, s[88:91], s67 offen\n" \
    "s_add_u32 s67, s85, 258048\n" \
    "buffer_load_dwordx4 a[12:15], v20, s[88:91], s67 offen\n" \
    "s_add_u32 s67, s85, 57344\n" \
    "buffer_load_dwordx4 a[16:19], v20, s[88:91], s67 offen\n" \
    "s_add_u32 s67, s85, 86016\n" \
    "buffer_load_dwordx4 a[20:23], v20, s[88:91], s67 offen\n" \
    "s_add_u32 s67, s85, 114688\n" \
    "buffer_load_dwordx4 a[24:27], v20, s[88:91], s67 offen\n" \
    "s_add_u32 s67, s85, 143360\n" \
    "buffer_load_dwordx4 a[28:31], v20, s[88:91], s67 offen\n" \
    "s_add_u32 s67, s85, 172032\n" \
    "buffer_load_dwordx4 a[32:35], v20, s[88:91], s67 offen\n" \
    "s_add_u32 s67, s85, 200704\n" \
    "buffer_load_dwordx4 a[36:39], v20, s[88:91], s67 offen\n" \
    "s_add_u32 s67, s85, 286720\n" \
    "buffer_load_dwordx4 a[40:43], v20, s[88:91], s67 offen\n" \
    "s_add_u32 s67, s85, 315392\n" \
    "buffer_load_dwordx4 a[44:47], v20, s[88:91], s67 offen\n" \
    "s_add_u32 s67, s85, 344064\n" \
    "buffer_load_dwordx4 a[48:51], v20, s[88:91], s67 offen\n" \
    "s_add_u32 s67, s85, 372736\n" \
    "buffer_load_dwordx4 a[52:55], v20, s[88:91], s67 offen\n" \
    "s_add_u32 s67, s85, 0\n" \
    "buffer_load_dwordx4 a[56:59], v21, s[88:91], s67 offen\n" \
    "s_add_u32 s67, s85, 28672\n" \
    "buffer_load_dwordx4 a[60:63], v21, s[88:91], s67 offen\n" \
    "s_add_u32 s67, s85, 229376\n" \
    "buffer_load_dwordx4 a[64:67], v21, s[88:91], s67 offen\n" \
    "s_add_u32 s67, s85, 258048\n" \
    "buffer_load_dwordx4 a[68:71], v21, s[88:91], s67 offen\n" \
    "s_add_u32 s67, s85, 57344\n" \
    "buffer_load_dwordx4 a[72:75], v21, s[88:91], s67 offen\n" \
    "s_add_u32 s67, s85, 86016\n" \
    "buffer_load_dwordx4 a[76:79], v21, s[88:91], s67 offen\n" \
    "s_add_u32 s67, s85, 114688\n" \
    "buffer_load_dwordx4 a[80:83], v21, s[88:91], s67 offen\n" \
    "s_add_u32 s67, s85, 143360\n" \
    "buffer_load_dwordx4 a[84:87], v21, s[88:91], s67 offen\n" \
    "s_add_u32 s67, s85, 172032\n" \
    "buffer_load_dwordx4 a[88:91], v21, s[88:91], s67 offen\n" \
    "s_add_u32 s67, s85, 200704\n" \
    "buffer_load_dwordx4 a[92:95], v21, s[88:91], s67 offen\n" \
    "s_add_u32 s67, s85, 286720\n" \
    "buffer_load_dwordx4 a[96:99], v21, s[88:91], s67 offen\n" \
    "s_add_u32 s67, s85, 315392\n" \
    "buffer_load_dwordx4 a[100:103], v21, s[88:91], s67 offen\n" \
    "s_add_u32 s67, s85, 344064\n" \
    "buffer_load_dwordx4 a[104:107], v21, s[88:91], s67 offen\n" \
    "s_add_u32 s67, s85, 372736\n" \
    "buffer_load_dwordx4 a[108:111], v21, s[88:91], s67 offen\n" \
    "s_add_u32 s67, s85, 1024\n" \
    "buffer_load_dwordx4 a[112:115], v20, s[88:91], s67 offen\n" \
    "s_add_u32 s67, s85, 29696\n" \
    "buffer_load_dwordx4 a[116:119], v20, s[88:91], s67 offen\n" \
    "s_add_u32 s67, s85, 230400\n" \
    "buffer_load_dwordx4 a[120:123], v20, s[88:91], s67 offen\n" \
    "s_add_u32 s67, s85, 259072\n" \
    "buffer_load_dwordx4 a[124:127], v20, s[88:91], s67 offen\n" \
    "s_add_u32 s67, s85, 58368\n" \
    "buffer_load_dwordx4 a[128:131], v20, s[88:91], s67 offen\n" \
    "s_add_u32 s67, s85, 87040\n" \
    "buffer_load_dwordx4 a[132:135], v20, s[88:91], s67 offen\n" \
    "s_add_u32 s67, s85, 115712\n" \
    "buffer_load_dwordx4 a[136:139], v20, s[88:91], s67 offen\n" \
    "s_add_u32 s67, s85, 144384\n" \
    "buffer_load_dwordx4 a[140:143], v20, s[88:91], s67 offen\n" \
    "s_add_u32 s67, s85, 173056\n" \
    "buffer_load_dwordx4 a[144:147], v20, s[88:91], s67 offen\n" \
    "s_add_u32 s67, s85, 201728\n" \
    "buffer_load_dwordx4 a[148:151], v20, s[88:91], s67 offen\n" \
    "s_add_u32 s67, s85, 287744\n" \
    "buffer_load_dwordx4 a[152:155], v20, s[88:91], s67 offen\n" \
    "s_add_u32 s67, s85, 316416\n" \
    "buffer_load_dwordx4 a[156:159], v20, s[88:91], s67 offen\n" \
    "s_add_u32 s67, s85, 345088\n" \
    "buffer_load_dwordx4 a[160:163], v20, s[88:91], s67 offen\n" \
    "s_add_u32 s67, s85, 373760\n" \
    "buffer_load_dwordx4 a[164:167], v20, s[88:91], s67 offen\n" \
    "s_add_u32 s67, s85, 6144\n" \
    "buffer_load_dwordx4 a[168:171], v20, s[88:91], s67 offen\n" \
    "s_add_u32 s67, s85, 34816\n" \
    "buffer_load_dwordx4 a[172:175], v20, s[88:91], s67 offen\n" \
    "s_add_u32 s67, s85, 63488\n" \
    "buffer_load_dwordx4 a[176:179], v20, s[88:91], s67 offen\n" \
    "s_add_u32 s67, s85, 92160\n" \
    "buffer_load_dwordx4 a[180:183], v20, s[88:91], s67 offen\n" \
    "s_add_u32 s67, s85, 120832\n" \
    "buffer_load_dwordx4 a[184:187], v20, s[88:91], s67 offen\n" \
    "s_add_u32 s67, s85, 149504\n" \
    "buffer_load_dwordx4 a[188:191], v20, s[88:91], s67 offen\n" \
    "s_add_u32 s67, s85, 178176\n" \
    "buffer_load_dwordx4 a[192:195], v20, s[88:91], s67 offen\n" \
    "s_add_u32 s67, s85, 206848\n" \
    "buffer_load_dwordx4 a[196:199], v20, s[88:91], s67 offen\n" \
    "s_waitcnt vmcnt(0)\n" \
    "s_add_u32 s67, s86, 0\n" \
    "buffer_load_dwordx4 a[200:203], v22, s[92:95], s67 offen\n" \
    "s_add_u32 s67, s86, 28672\n" \
    "buffer_load_dwordx4 a[204:207], v22, s[92:95], s67 offen\n" \
    "s_add_u32 s67, s86, 229376\n" \
    "buffer_load_dwordx4 a[208:211], v22, s[92:95], s67 offen\n" \
    "s_add_u32 s67, s86, 258048\n" \
    "buffer_load_dwordx4 a[212:215], v22, s[92:95], s67 offen\n" \
    "s_add_u32 s67, s86, 57344\n" \
    "buffer_load_dwordx4 a[216:219], v22, s[92:95], s67 offen\n" \
    "s_add_u32 s67, s86, 86016\n" \
    "buffer_load_dwordx4 a[220:223], v22, s[92:95], s67 offen\n" \
    "s_add_u32 s67, s86, 114688\n" \
    "buffer_load_dwordx4 a[224:227], v22, s[92:95], s67 offen\n" \
    "s_add_u32 s67, s86, 143360\n" \
    "buffer_load_dwordx4 a[228:231], v22, s[92:95], s67 offen\n" \
    "s_add_u32 s67, s86, 172032\n" \
    "buffer_load_dwordx4 a[232:235], v22, s[92:95], s67 offen\n" \
    "s_add_u32 s67, s86, 200704\n" \
    "buffer_load_dwordx4 a[236:239], v22, s[92:95], s67 offen\n" \
    "s_add_u32 s67, s86, 286720\n" \
    "buffer_load_dwordx4 a[240:243], v22, s[92:95], s67 offen\n" \
    "s_add_u32 s67, s86, 315392\n" \
    "buffer_load_dwordx4 a[244:247], v22, s[92:95], s67 offen\n" \
    "s_add_u32 s67, s86, 344064\n" \
    "buffer_load_dwordx4 a[248:251], v22, s[92:95], s67 offen\n" \
    "s_add_u32 s67, s86, 372736\n" \
    "buffer_load_dwordx4 a[252:255], v22, s[92:95], s67 offen\n" \
    "s_add_u32 s67, s86, 401408\n" \
    "buffer_load_dwordx4 v[192:195], v22, s[92:95], s67 offen\n" \
    "s_add_u32 s67, s86, 430080\n" \
    "buffer_load_dwordx4 v[196:199], v22, s[92:95], s67 offen\n" \
    "s_add_u32 s67, s86, 630784\n" \
    "buffer_load_dwordx4 v[200:203], v22, s[92:95], s67 offen\n" \
    "s_add_u32 s67, s86, 659456\n" \
    "buffer_load_dwordx4 v[204:207], v22, s[92:95], s67 offen\n" \
    "s_add_u32 s67, s86, 458752\n" \
    "buffer_load_dwordx4 v[208:211], v22, s[92:95], s67 offen\n" \
    "s_add_u32 s67, s86, 487424\n" \
    "buffer_load_dwordx4 v[212:215], v22, s[92:95], s67 offen\n" \
    "s_add_u32 s67, s86, 516096\n" \
    "buffer_load_dwordx4 v[216:219], v22, s[92:95], s67 offen\n" \
    "s_add_u32 s67, s86, 544768\n" \
    "buffer_load_dwordx4 v[220:223], v22, s[92:95], s67 offen\n" \
    "s_add_u32 s67, s86, 573440\n" \
    "buffer_load_dwordx4 v[224:227], v22, s[92:95], s67 offen\n" \
    "s_add_u32 s67, s86, 602112\n" \
    "buffer_load_dwordx4 v[228:231], v22, s[92:95], s67 offen\n" \
    "s_add_u32 s67, s86, 688128\n" \
    "buffer_load_dwordx4 v[232:235], v22, s[92:95], s67 offen\n" \
    "s_add_u32 s67, s86, 716800\n" \
    "buffer_load_dwordx4 v[236:239], v22, s[92:95], s67 offen\n" \
    "s_add_u32 s67, s86, 745472\n" \
    "buffer_load_dwordx4 v[240:243], v22, s[92:95], s67 offen\n" \
    "s_add_u32 s67, s86, 774144\n" \
    "buffer_load_dwordx4 v[244:247], v22, s[92:95], s67 offen\n" \
    "s_add_u32 s67, s86, 802816\n" \
    "buffer_load_dwordx4 v[152:155], v22, s[92:95], s67 offen\n" \
    "s_add_u32 s67, s86, 831488\n" \
    "buffer_load_dwordx4 v[156:159], v22, s[92:95], s67 offen\n" \
    "s_add_u32 s67, s86, 1032192\n" \
    "buffer_load_dwordx4 v[160:163], v22, s[92:95], s67 offen\n" \
    "s_add_u32 s67, s86, 1060864\n" \
    "buffer_load_dwordx4 v[164:167], v22, s[92:95], s67 offen\n" \
    "s_add_u32 s67, s86, 860160\n" \
    "buffer_load_dwordx4 v[168:171], v22, s[92:95], s67 offen\n" \
    "s_add_u32 s67, s86, 888832\n" \
    "buffer_load_dwordx4 v[172:175], v22, s[92:95], s67 offen\n" \
    "s_add_u32 s67, s86, 917504\n" \
    "buffer_load_dwordx4 v[176:179], v22, s[92:95], s67 offen\n" \
    "s_add_u32 s67, s86, 946176\n" \
    "buffer_load_dwordx4 v[180:183], v22, s[92:95], s67 offen\n" \
    "s_add_u32 s67, s86, 974848\n" \
    "buffer_load_dwordx4 v[184:187], v22, s[92:95], s67 offen\n" \
    "s_add_u32 s67, s86, 1003520\n" \
    "buffer_load_dwordx4 v[188:191], v22, s[92:95], s67 offen\n" \
    "s_waitcnt vmcnt(0)\n" \
    "ds_write_b128 v17, v[152:155]\n" \
    "ds_write_b128 v17, v[156:159] offset:1024\n" \
    "ds_write_b128 v17, v[160:163] offset:2048\n" \
    "ds_write_b128 v17, v[164:167] offset:3072\n" \
    "ds_write_b128 v17, v[168:171] offset:4096\n" \
    "ds_write_b128 v17, v[172:175] offset:5120\n" \
    "ds_write_b128 v17, v[176:179] offset:6144\n" \
    "ds_write_b128 v17, v[180:183] offset:7168\n" \
    "ds_write_b128 v17, v[184:187] offset:8192\n" \
    "ds_write_b128 v17, v[188:191] offset:9216\n" \
    "s_waitcnt lgkmcnt(0)\n" \
    "s_add_u32 s67, s86, 1089536\n" \
    "buffer_load_dwordx4 v[152:155], v22, s[92:95], s67 offen\n" \
    "s_add_u32 s67, s86, 1118208\n" \
    "buffer_load_dwordx4 v[156:159], v22, s[92:95], s67 offen\n" \
    "s_add_u32 s67, s86, 1146880\n" \
    "buffer_load_dwordx4 v[160:163], v22, s[92:95], s67 offen\n" \
    "s_add_u32 s67, s86, 1175552\n" \
    "buffer_load_dwordx4 v[164:167], v22, s[92:95], s67 offen\n" \
    "s_add_u32 s67, s86, 1204224\n" \
    "buffer_load_dwordx4 v[168:171], v22, s[92:95], s67 offen\n" \
    "s_add_u32 s67, s86, 1232896\n" \
    "buffer_load_dwordx4 v[172:175], v22, s[92:95], s67 offen\n" \
    "s_add_u32 s67, s86, 1433600\n" \
    "buffer_load_dwordx4 v[176:179], v22, s[92:95], s67 offen\n" \
    "s_add_u32 s67, s86, 1462272\n" \
    "buffer_load_dwordx4 v[180:183], v22, s[92:95], s67 offen\n" \
    "s_add_u32 s67, s86, 1261568\n" \
    "buffer_load_dwordx4 v[184:187], v22, s[92:95], s67 offen\n" \
    "s_add_u32 s67, s86, 1290240\n" \
    "buffer_load_dwordx4 v[188:191], v22, s[92:95], s67 offen\n" \
    "s_waitcnt vmcnt(0)\n" \
    "ds_write_b128 v17, v[152:155] offset:10240\n" \
    "ds_write_b128 v17, v[156:159] offset:11264\n" \
    "ds_write_b128 v17, v[160:163] offset:12288\n" \
    "ds_write_b128 v17, v[164:167] offset:13312\n" \
    "ds_write_b128 v17, v[168:171] offset:14336\n" \
    "ds_write_b128 v17, v[172:175] offset:15360\n" \
    "ds_write_b128 v17, v[176:179] offset:16384\n" \
    "ds_write_b128 v17, v[180:183] offset:17408\n" \
    "ds_write_b128 v17, v[184:187] offset:18432\n" \
    "ds_write_b128 v17, v[188:191] offset:19456\n" \
    "s_waitcnt lgkmcnt(0)\n" \
    "s_add_u32 s67, s86, 1318912\n" \
    "buffer_load_dwordx4 v[152:155], v22, s[92:95], s67 offen\n" \
    "s_add_u32 s67, s86, 1347584\n" \
    "buffer_load_dwordx4 v[156:159], v22, s[92:95], s67 offen\n" \
    "s_add_u32 s67, s86, 1376256\n" \
    "buffer_load_dwordx4 v[160:163], v22, s[92:95], s67 offen\n" \
    "s_add_u32 s67, s86, 1404928\n" \
    "buffer_load_dwordx4 v[164:167], v22, s[92:95], s67 offen\n" \
    "s_add_u32 s67, s86, 1490944\n" \
    "buffer_load_dwordx4 v[168:171], v22, s[92:95], s67 offen\n" \
    "s_add_u32 s67, s86, 1519616\n" \
    "buffer_load_dwordx4 v[172:175], v22, s[92:95], s67 offen\n" \
    "s_add_u32 s67, s86, 1548288\n" \
    "buffer_load_dwordx4 v[176:179], v22, s[92:95], s67 offen\n" \
    "s_add_u32 s67, s86, 1576960\n" \
    "buffer_load_dwordx4 v[180:183], v22, s[92:95], s67 offen\n" \
    "s_waitcnt vmcnt(0)\n" \
    "ds_write_b128 v17, v[152:155] offset:20480\n" \
    "ds_write_b128 v17, v[156:159] offset:21504\n" \
    "ds_write_b128 v17, v[160:163] offset:22528\n" \
    "ds_write_b128 v17, v[164:167] offset:23552\n" \
    "ds_write_b128 v17, v[168:171] offset:24576\n" \
    "ds_write_b128 v17, v[172:175] offset:25600\n" \
    "ds_write_b128 v17, v[176:179] offset:26624\n" \
    "ds_write_b128 v17, v[180:183] offset:27648\n" \
    "s_waitcnt lgkmcnt(0)\n" \
    "s_mov_b32 s87, 0\n" \
    "Lt0W0_%=:\n" \
    "v_mov_b32 v248, 0\n" \
    "global_load_dword v254, v248, s[40:41] sc1\n" \
    "s_waitcnt vmcnt(0)\n" \
    "v_readfirstlane_b32 s67, v254\n" \
    "s_nop 1\n" \
    "s_cmp_ge_u32 s67, 160\n" \
    "s_cbranch_scc1 Lt0dW0_%=\n" \
    "s_sleep 4\n" \
    "s_add_u32 s87, s87, 1\n" \
    "s_cmp_lt_u32 s87, 0x40000\n" \
    "s_cbranch_scc1 Lt0W0_%=\n" \
    "Lt0dW0_%=:\n" \
    "s_mov_b32 s87, 0\n" \
    "Lp0W0_%=:\n" \
    "v_mov_b32 v248, 45120\n" \
    "global_load_dword v254, v248, s[40:41] sc1\n" \
    "s_waitcnt vmcnt(0)\n" \
    "v_readfirstlane_b32 s72, v254\n" \
    "s_nop 1\n" \
    "s_cmp_lg_u32 s72, -1\n" \
    "s_cbranch_scc1 Lp0dW0_%=\n" \
    "s_add_u32 s87, s87, 1\n" \
    "s_cmp_lt_u32 s87, 0x100000\n" \
    "s_cbranch_scc1 Lp0W0_%=\n" \
    "s_mov_b32 s72, 0\n" \
    "Lp0dW0_%=:\n" \
    "s_and_b32 s61, s72, 0x2000\n" \
    "s_add_u32 s62, s61, 65536\n" \
    "s_waitcnt vmcnt(0)\n" \
    "s_waitcnt lgkmcnt(0)\n" \
    "s_barrier\n" \
    "s_memrealtime s[96:97]\n" \
    "s_waitcnt lgkmcnt(0)\n" \
    "LloopW0_%=:\n" \
    "buffer_load_dwordx4 v[26:29], v3, s[36:39], s61 offen sc1\n" \
    "buffer_load_dwordx4 v[30:33], v4, s[36:39], s61 offen sc1\n" \
    "v_mov_b32 v54, 0\n" \
    "v_mov_b32 v55, 0\n" \
    "v_mov_b32 v56, 0\n" \
    "v_mov_b32 v57, 0\n" \
    "v_mov_b32 v58, 0\n" \
    "v_mov_b32 v59, 0\n" \
    "v_mov_b32 v60, 0\n" \
    "v_mov_b32 v61, 0\n" \
    "v_mov_b32 v62, 0\n" \
    "v_mov_b32 v63, 0\n" \
    "v_mov_b32 v64, 0\n" \
    "v_mov_b32 v65, 0\n" \
    "v_mov_b32 v66, 0\n" \
    "v_mov_b32 v67, 0\n" \
    "v_mov_b32 v68, 0\n" \
    "v_mov_b32 v69, 0\n" \
    "s_nop 15\n" \
    "s_nop 15\n" \
    "s_nop 7\n" \
    "buffer_load_dwordx4 v[26:29], v3, s[36:39], s61 offen sc1\n" \
    "buffer_load_dwordx4 v[30:33], v4, s[36:39], s61 offen sc1\n" \
    "s_waitcnt vmcnt(2)\n" \
    "v_max3_u32 v248, v26, v27, v28\n" \
    "v_max3_u32 v249, v29, v30, v31\n" \
    "v_max3_u32 v248, v248, v32, v33\n" \
    "v_max_u32 v248, v248, v249\n" \
    "v_cmp_eq_u32 vcc, -1, v248\n" \
    "s_cbranch_vccz LokMW0_%=\n" \
    "s_add_u32 s84, s84, 1\n" \
    "s_waitcnt vmcnt(0)\n" \
    "v_max3_u32 v248, v26, v27, v28\n" \
    "v_max3_u32 v249, v29, v30, v31\n" \
    "v_max3_u32 v248, v248, v32, v33\n" \
    "v_max_u32 v248, v248, v249\n" \
    "v_cmp_eq_u32 vcc, -1, v248\n" \
    "s_cbranch_vccz LokMW0_%=\n" \
    "s_add_u32 s84, s84, 1\n" \
    "s_branch LslowMW0_%=\n" \
    "LokMW0_%=:\n" \
    "s_nop 0\n" \
    "ds_write_b128 v5, v[26:29]\n" \
    "ds_write_b128 v5, v[30:33] offset:4096\n" \
    "v_smfmac_f32_16x16x64_bf16 v[54:57], v[26:29], a[0:7], v1\n" \
    "v_smfmac_f32_16x16x64_bf16 v[58:61], v[26:29], a[56:63], v1\n" \
    "v_smfmac_f32_16x16x64_bf16 v[62:65], v[26:29], a[112:119], v1\n" \
    "v_smfmac_f32_16x16x64_bf16 v[66:69], v[26:29], a[168:175], v1\n" \
    "s_waitcnt lgkmcnt(0)\n" \
    "s_barrier\n" \
    "ds_read_b128 v[34:37], v5 offset:1024\n" \
    "ds_read_b128 v[38:41], v5 offset:2048\n" \
    "ds_read_b128 v[42:45], v5 offset:3072\n" \
    "ds_read_b128 v[46:49], v5 offset:5120\n" \
    "ds_read_b128 v[50:53], v5 offset:6144\n" \
    "v_smfmac_f32_16x16x64_bf16 v[54:57], v[30:33], a[8:15], v1\n" \
    "v_smfmac_f32_16x16x64_bf16 v[58:61], v[30:33], a[64:71], v1\n" \
    "v_smfmac_f32_16x16x64_bf16 v[62:65], v[30:33], a[120:127], v1\n" \
    "s_waitcnt lgkmcnt(4)\n" \
    "v_smfmac_f32_16x16x64_bf16 v[54:57], v[34:37], a[16:23], v1\n" \
    "v_smfmac_f32_16x16x64_bf16 v[58:61], v[34:37], a[72:79], v1\n" \
    "v_smfmac_f32_16x16x64_bf16 v[62:65], v[34:37], a[128:135], v1\n" \
    "buffer_load_dwordx4 v[86:89], v0, s[44:47], s63 offen sc1\n" \
    "v_smfmac_f32_16x16x64_bf16 v[66:69], v[34:37], a[176:183], v1\n" \
    "s_waitcnt lgkmcnt(3)\n" \
    "v_smfmac_f32_16x16x64_bf16 v[66:69], v[38:41], a[184:191], v1\n" \
    "v_mov_b32 v138, 0\n" \
    "v_mov_b32 v139, 0\n" \
    "v_mov_b32 v140, 0\n" \
    "s_waitcnt lgkmcnt(2)\n" \
    "v_smfmac_f32_16x16x64_bf16 v[66:69], v[42:45], a[192:199], v1\n" \
    "ds_read_b128 v[102:105], v9\n" \
    "ds_read_b128 v[106:109], v9 offset:512\n" \
    "ds_read_b128 v[110:113], v9 offset:1024\n" \
    "v_smfmac_f32_16x16x64_bf16 v[54:57], v[38:41], a[24:31], v1\n" \
    "buffer_load_dwordx4 v[90:93], v0, s[44:47], s63 offen offset:1024 sc1\n" \
    "ds_read_b128 v[114:117], v9 offset:1536\n" \
    "ds_read_b128 v[118:121], v9 offset:2048\n" \
    "ds_read_b128 v[122:125], v9 offset:2560\n" \
    "v_smfmac_f32_16x16x64_bf16 v[54:57], v[42:45], a[32:39], v1\n" \
    "ds_read_b128 v[126:129], v9 offset:3072\n" \
    "ds_read_b128 v[130:133], v9 offset:3072\n" \
    "ds_read_b128 v[152:155], v17\n" \
    "s_waitcnt lgkmcnt(10)\n" \
    "v_smfmac_f32_16x16x64_bf16 v[54:57], v[46:49], a[40:47], v1\n" \
    "v_add_f32 v250, v66, v67\n" \
    "v_add_f32 v251, v68, v69\n" \
    "v_cvt_pk_bf16_f32 v250, v250, v251\n" \
    "s_waitcnt lgkmcnt(9)\n" \
    "v_smfmac_f32_16x16x64_bf16 v[54:57], v[50:53], a[48:55], v1\n" \
    "ds_write_b32 v7, v250\n" \
    "ds_read_b128 v[156:159], v17 offset:1024\n" \
    "ds_read_b128 v[160:163], v17 offset:2048\n" \
    "v_smfmac_f32_16x16x64_bf16 v[58:61], v[38:41], a[80:87], v1\n" \
    "buffer_load_dwordx4 v[94:97], v0, s[44:47], s63 offen offset:2048 sc1\n" \
    "ds_read_b128 v[164:167], v17 offset:3072\n" \
    "ds_read_b128 v[168:171], v17 offset:4096\n" \
    "ds_read_b128 v[172:175], v17 offset:5120\n" \
    "v_smfmac_f32_16x16x64_bf16 v[58:61], v[42:45], a[88:95], v1\n" \
    "ds_read_b128 v[176:179], v17 offset:6144\n" \
    "ds_read_b128 v[180:183], v17 offset:7168\n" \
    "ds_read_b128 v[184:187], v17 offset:8192\n" \
    "v_smfmac_f32_16x16x64_bf16 v[58:61], v[46:49], a[96:103], v1\n" \
    "v_add_f32 v248, v54, v55\n" \
    "v_add_f32 v249, v56, v57\n" \
    "v_cvt_pk_bf16_f32 v248, v248, v249\n" \
    "v_smfmac_f32_16x16x64_bf16 v[58:61], v[50:53], a[104:111], v1\n" \
    "ds_write_b32 v6, v248\n" \
    "ds_read_b128 v[188:191], v17 offset:9216\n" \
    "v_mov_b32 v141, 0\n" \
    "v_smfmac_f32_16x16x64_bf16 v[62:65], v[38:41], a[136:143], v1\n" \
    "buffer_load_dwordx4 v[98:101], v0, s[44:47], s63 offen offset:3072 sc1\n" \
    "v_smfmac_f32_16x16x64_bf16 v[62:65], v[42:45], a[144:151], v1\n" \
    "v_smfmac_f32_16x16x64_bf16 v[62:65], v[46:49], a[152:159], v1\n" \
    "s_nop 0\n" \
    "v_add_f32 v250, v58, v59\n" \
    "v_add_f32 v251, v60, v61\n" \
    "v_cvt_pk_bf16_f32 v250, v250, v251\n" \
    "v_smfmac_f32_16x16x64_bf16 v[62:65], v[50:53], a[160:167], v1\n" \
    "ds_write_b32 v6, v250 offset:256\n" \
    "s_nop 6\n" \
    "v_add_f32 v248, v62, v63\n" \
    "v_add_f32 v249, v64, v65\n" \
    "v_cvt_pk_bf16_f32 v248, v248, v249\n" \
    "ds_write_b32 v6, v248 offset:512\n" \
    "s_waitcnt lgkmcnt(0)\n" \
    "s_barrier\n" \
    "ds_read_b64_tr_b16 v[70:71], v8\n" \
    "ds_read_b64_tr_b16 v[72:73], v8 offset:32\n" \
    "ds_read_b64_tr_b16 v[74:75], v8 offset:1024\n" \
    "ds_read_b64_tr_b16 v[76:77], v8 offset:1056\n" \
    "ds_read_b64_tr_b16 v[78:79], v8 offset:2048\n" \
    "ds_read_b64_tr_b16 v[80:81], v8 offset:2080\n" \
    "ds_read_b64_tr_b16 v[82:83], v8 offset:3072\n" \
    "ds_read_b64_tr_b16 v[84:85], v8 offset:3104\n" \
    "s_waitcnt lgkmcnt(6)\n" \
    "v_smfmac_f32_16x16x64_bf16 v[138:141], v[70:73], v[102:109], v2\n" \
    "s_waitcnt lgkmcnt(4)\n" \
    "v_smfmac_f32_16x16x64_bf16 v[138:141], v[74:77], v[110:117], v2\n" \
    "s_waitcnt lgkmcnt(2)\n" \
    "v_smfmac_f32_16x16x64_bf16 v[138:141], v[78:81], v[118:125], v2\n" \
    "s_waitcnt lgkmcnt(0)\n" \
    "v_smfmac_f32_16x16x64_bf16 v[138:141], v[82:85], v[126:133], v2\n" \
    "s_nop 7\n" \
    "v_permlane32_swap_b32 v138, v140\n" \
    "v_permlane32_swap_b32 v139, v141\n" \
    "v_add_f32 v138, v138, v140\n" \
    "v_add_f32 v139, v139, v141\n" \
    "v_cvt_pk_bf16_f32 v138, v138, v139\n" \
    "v_min_u32 v138, -2, v138\n" \
    "s_mov_b64 exec, s[74:75]\n" \
    "buffer_store_dword v138, v10, s[36:39], s61 offen sc1\n" \
    "s_mov_b64 exec, -1\n" \
    "ds_read_b64_tr_b16 v[118:119], v18\n" \
    "ds_read_b64_tr_b16 v[120:121], v18 offset:64\n" \
    "v_mov_b32 v70, 0\n" \
    "v_mov_b32 v71, 0\n" \
    "v_mov_b32 v72, 0\n" \
    "v_mov_b32 v73, 0\n" \
    "v_mov_b32 v74, 0\n" \
    "v_mov_b32 v75, 0\n" \
    "v_mov_b32 v76, 0\n" \
    "v_mov_b32 v77, 0\n" \
    "v_mov_b32 v78, 0\n" \
    "v_mov_b32 v79, 0\n" \
    "v_mov_b32 v80, 0\n" \
    "v_mov_b32 v81, 0\n" \
    "v_mov_b32 v82, 0\n" \
    "v_mov_b32 v83, 0\n" \
    "v_mov_b32 v84, 0\n" \
    "v_mov_b32 v85, 0\n" \
    "v_smfmac_f32_16x16x64_bf16 v[70:73], v[26:29], a[200:207], v1\n" \
    "v_smfmac_f32_16x16x64_bf16 v[78:81], v[26:29], v[152:159], v1\n" \
    "ds_read_b128 v[152:155], v17 offset:10240\n" \
    "ds_read_b128 v[156:159], v17 offset:11264\n" \
    "v_smfmac_f32_16x16x64_bf16 v[70:73], v[30:33], a[208:215], v1\n" \
    "v_smfmac_f32_16x16x64_bf16 v[78:81], v[30:33], v[160:167], v1\n" \
    "ds_read_b128 v[160:163], v17 offset:12288\n" \
    "ds_read_b128 v[164:167], v17 offset:13312\n" \
    "s_add_u32 s67, s60, 1\n" \
    "s_lshr_b32 s67, s67, 2\n" \
    "s_min_u32 s67, s67, 15\n" \
    "s_lshl_b32 s67, s67, 8\n" \
    "v_mov_b32 v253, s67\n" \
    "global_load_dword v254, v253, s[40:41] sc1\n" \
    "v_smfmac_f32_16x16x64_bf16 v[70:73], v[34:37], a[216:223], v1\n" \
    "v_smfmac_f32_16x16x64_bf16 v[78:81], v[34:37], v[168:175], v1\n" \
    "ds_read_b128 v[168:171], v17 offset:14336\n" \
    "ds_read_b128 v[172:175], v17 offset:15360\n" \
    "v_smfmac_f32_16x16x64_bf16 v[70:73], v[38:41], a[224:231], v1\n" \
    "v_smfmac_f32_16x16x64_bf16 v[78:81], v[38:41], v[176:183], v1\n" \
    "ds_read_b128 v[176:179], v17 offset:16384\n" \
    "ds_read_b128 v[180:183], v17 offset:17408\n" \
    "v_smfmac_f32_16x16x64_bf16 v[70:73], v[42:45], a[232:239], v1\n" \
    "v_smfmac_f32_16x16x64_bf16 v[78:81], v[42:45], v[184:191], v1\n" \
    "ds_read_b128 v[184:187], v17 offset:18432\n" \
    "ds_read_b128 v[188:191], v17 offset:19456\n" \
    "v_smfmac_f32_16x16x64_bf16 v[70:73], v[46:49], a[240:247], v1\n" \
    "s_waitcnt lgkmcnt(8)\n" \
    "v_smfmac_f32_16x16x64_bf16 v[78:81], v[46:49], v[152:159], v1\n" \
    "ds_read_b128 v[152:155], v17 offset:20480\n" \
    "ds_read_b128 v[156:159], v17 offset:21504\n" \
    "v_smfmac_f32_16x16x64_bf16 v[70:73], v[50:53], a[248:255], v1\n" \
    "s_waitcnt lgkmcnt(8)\n" \
    "v_smfmac_f32_16x16x64_bf16 v[78:81], v[50:53], v[160:167], v1\n" \
    "ds_read_b128 v[160:163], v17 offset:22528\n" \
    "ds_read_b128 v[164:167], v17 offset:23552\n" \
    "v_smfmac_f32_16x16x64_bf16 v[74:77], v[26:29], v[192:199], v1\n" \
    "s_waitcnt lgkmcnt(8)\n" \
    "v_smfmac_f32_16x16x64_bf16 v[82:85], v[26:29], v[168:175], v1\n" \
    "ds_read_b128 v[168:171], v17 offset:24576\n" \
    "ds_read_b128 v[172:175], v17 offset:25600\n" \
    "v_smfmac_f32_16x16x64_bf16 v[74:77], v[30:33], v[200:207], v1\n" \
    "s_waitcnt lgkmcnt(8)\n" \
    "v_smfmac_f32_16x16x64_bf16 v[82:85], v[30:33], v[176:183], v1\n" \
    "ds_read_b128 v[176:179], v17 offset:26624\n" \
    "ds_read_b128 v[180:183], v17 offset:27648\n" \
    "v_smfmac_f32_16x16x64_bf16 v[74:77], v[34:37], v[208:215], v1\n" \
    "s_waitcnt lgkmcnt(8)\n" \
    "v_smfmac_f32_16x16x64_bf16 v[82:85], v[34:37], v[184:191], v1\n" \
    "v_add_f32 v70, v70, v71\n" \
    "v_add_f32 v72, v72, v73\n" \
    "v_cvt_pk_bf16_f32 v144, v70, v72\n" \
    "v_smfmac_f32_16x16x64_bf16 v[74:77], v[38:41], v[216:223], v1\n" \
    "s_waitcnt lgkmcnt(6)\n" \
    "v_smfmac_f32_16x16x64_bf16 v[82:85], v[38:41], v[152:159], v1\n" \
    "v_add_f32 v78, v78, v79\n" \
    "v_add_f32 v80, v80, v81\n" \
    "v_cvt_pk_bf16_f32 v146, v78, v80\n" \
    "v_smfmac_f32_16x16x64_bf16 v[74:77], v[42:45], v[224:231], v1\n" \
    "s_waitcnt lgkmcnt(4)\n" \
    "v_smfmac_f32_16x16x64_bf16 v[82:85], v[42:45], v[160:167], v1\n" \
    "v_smfmac_f32_16x16x64_bf16 v[74:77], v[46:49], v[232:239], v1\n" \
    "s_waitcnt lgkmcnt(2)\n" \
    "v_smfmac_f32_16x16x64_bf16 v[82:85], v[46:49], v[168:175], v1\n" \
    "v_smfmac_f32_16x16x64_bf16 v[74:77], v[50:53], v[240:247], v1\n" \
    "s_waitcnt lgkmcnt(0)\n" \
    "v_smfmac_f32_16x16x64_bf16 v[82:85], v[50:53], v[176:183], v1\n" \
    "buffer_load_dwordx4 v[134:137], v13, s[36:39], s61 offen sc1\n" \
    "s_nop 4\n" \
    "v_add_f32 v74, v74, v75\n" \
    "v_add_f32 v76, v76, v77\n" \
    "v_cvt_pk_bf16_f32 v145, v74, v76\n" \
    "v_add_f32 v82, v82, v83\n" \
    "v_add_f32 v84, v84, v85\n" \
    "v_cvt_pk_bf16_f32 v147, v82, v84\n" \
    "v_permlane32_swap_b32 v144, v146\n" \
    "s_nop 0\n" \
    "v_permlane32_swap_b32 v145, v147\n" \
    "s_nop 1\n" \
    "v_permlane16_swap_b32 v144, v145\n" \
    "v_permlane16_swap_b32 v146, v147\n" \
    "s_cmp_eq_u32 s69, 0\n" \
    "s_cbranch_scc1 LnoVMW0_%=\n" \
    "buffer_store_dwordx4 v[144:147], v0, s[52:55], s65 offen sc1\n" \
    "LnoVMW0_%=:\n" \
    "s_nop 15\n" \
    "s_nop 3\n" \
    "buffer_load_dwordx4 v[102:105], v11, s[36:39], s61 offen sc1\n" \
    "buffer_load_dwordx4 v[110:113], v11, s[36:39], s61 offen offset:1024 sc1\n" \
    "buffer_load_dwordx2 v[142:143], v12, s[36:39], s61 offen sc1\n" \
    "s_nop 7\n" \
    "buffer_store_dwordx4 v[118:121], v19, s[48:51], s64 offen sc1\n" \
    "s_nop 15\n" \
    "s_nop 15\n" \
    "s_nop 15\n" \
    "s_nop 7\n" \
    "buffer_load_dwordx4 v[134:137], v13, s[36:39], s61 offen sc1\n" \
    "s_cmp_eq_u32 s60, 63\n" \
    "s_cbranch_scc1 LskipW0_%=\n" \
    "s_waitcnt vmcnt(6)\n" \
    "v_max3_u32 v248, v134, v135, v136\n" \
    "v_max_u32 v248, v248, v137\n" \
    "v_cmp_eq_u32 vcc, -1, v248\n" \
    "s_cbranch_vccz LokPW0_%=\n" \
    "s_add_u32 s84, s84, 0x100000\n" \
    "s_waitcnt vmcnt(0)\n" \
    "v_max3_u32 v248, v134, v135, v136\n" \
    "v_max_u32 v248, v248, v137\n" \
    "v_cmp_eq_u32 vcc, -1, v248\n" \
    "s_cbranch_vccz LokPW0_%=\n" \
    "s_add_u32 s84, s84, 0x100000\n" \
    "s_branch LslowPW0_%=\n" \
    "LokPW0_%=:\n" \
    "s_nop 0\n" \
    "v_cndmask_b32 v62, v136, v134, s[58:59]\n" \
    "v_cndmask_b32 v63, v137, v135, s[58:59]\n" \
    "v_cndmask_b32 v64, v134, v136, s[58:59]\n" \
    "v_cndmask_b32 v65, v135, v137, s[58:59]\n" \
    "v_mov_b32_dpp v62, v62 row_ror:8 row_mask:0xf bank_mask:0xf bound_ctrl:1\n" \
    "v_mov_b32_dpp v63, v63 row_ror:8 row_mask:0xf bank_mask:0xf bound_ctrl:1\n" \
    "v_lshlrev_b32 v54, 16, v64\n" \
    "v_and_b32 v55, 0xffff0000, v64\n" \
    "v_lshlrev_b32 v56, 16, v65\n" \
    "v_and_b32 v57, 0xffff0000, v65\n" \
    "v_lshlrev_b32 v58, 16, v62\n" \
    "v_and_b32 v59, 0xffff0000, v62\n" \
    "v_lshlrev_b32 v60, 16, v63\n" \
    "v_and_b32 v61, 0xffff0000, v63\n" \
    "v_pk_add_f32 v[54:55], v[54:55], v[58:59]\n" \
    "v_pk_add_f32 v[56:57], v[56:57], v[60:61]\n" \
    "v_max_f32 v66, v54, v55\n" \
    "v_max3_f32 v66, v66, v56, v57\n" \
    "s_nop 1\n" \
    "v_max_f32_dpp v66, v66, v66 row_ror:8 row_mask:0xf bank_mask:0xf\n" \
    "v_pk_add_f32 v[62:63], v[54:55], v[66:67] op_sel_hi:[1,0] neg_lo:[0,1] neg_hi:[0,1]\n" \
    "v_pk_add_f32 v[64:65], v[56:57], v[66:67] op_sel_hi:[1,0] neg_lo:[0,1] neg_hi:[0,1]\n" \
    "v_exp_f32 v62, v62\n" \
    "v_exp_f32 v63, v63\n" \
    "v_exp_f32 v64, v64\n" \
    "v_exp_f32 v65, v65\n" \
    "s_nop 0\n" \
    "v_pk_add_f32 v[248:249], v[62:63], v[64:65]\n" \
    "v_add_f32 v68, v248, v249\n" \
    "s_nop 1\n" \
    "v_add_f32_dpp v68, v68, v68 row_ror:8 row_mask:0xf bank_mask:0xf\n" \
    "s_waitcnt vmcnt(2)\n" \
    "v_max3_u32 v248, v102, v103, v104\n" \
    "v_max3_u32 v249, v105, v110, v111\n" \
    "v_max3_u32 v248, v248, v112, v113\n" \
    "v_max3_u32 v249, v249, v142, v143\n" \
    "v_max_u32 v248, v248, v249\n" \
    "v_cmp_eq_u32 vcc, -1, v248\n" \
    "s_cbranch_vccz LokEW0_%=\n" \
    "s_add_u32 s84, s84, 0x400\n" \
    "s_branch LslowEW0_%=\n" \
    "LokEW0_%=:\n" \
    "s_nop 0\n" \
    "v_mov_b32_dpp v106, v102 row_ror:8 row_mask:0xf bank_mask:0xf bound_ctrl:1\n" \
    "v_mov_b32_dpp v107, v103 row_ror:8 row_mask:0xf bank_mask:0xf bound_ctrl:1\n" \
    "v_mov_b32_dpp v108, v104 row_ror:8 row_mask:0xf bank_mask:0xf bound_ctrl:1\n" \
    "v_mov_b32_dpp v109, v105 row_ror:8 row_mask:0xf bank_mask:0xf bound_ctrl:1\n" \
    "v_mfma_f32_16x16x32_bf16 v[118:121], v[86:89], v[102:105], 0\n" \
    "v_max_f32 v58, v66, v142\n" \
    "v_mfma_f32_16x16x32_bf16 v[126:129], v[94:97], v[110:113], 0\n" \
    "v_sub_f32 v59, v66, v58\n" \
    "v_sub_f32 v60, v142, v58\n" \
    "v_mfma_f32_16x16x32_bf16 v[122:125], v[90:93], v[106:109], 0\n" \
    "v_exp_f32 v59, v59\n" \
    "v_exp_f32 v60, v60\n" \
    "v_mov_b32_dpp v114, v110 row_ror:8 row_mask:0xf bank_mask:0xf bound_ctrl:1\n" \
    "v_mov_b32_dpp v115, v111 row_ror:8 row_mask:0xf bank_mask:0xf bound_ctrl:1\n" \
    "v_mov_b32_dpp v116, v112 row_ror:8 row_mask:0xf bank_mask:0xf bound_ctrl:1\n" \
    "v_mov_b32_dpp v117, v113 row_ror:8 row_mask:0xf bank_mask:0xf bound_ctrl:1\n" \
    "v_mul_f32 v61, v59, v68\n" \
    "s_nop 0\n" \
    "v_mfma_f32_16x16x32_bf16 v[130:133], v[98:101], v[114:117], 0\n" \
    "v_fmac_f32 v61, v60, v143\n" \
    "v_rcp_f32 v61, v61\n" \
    "s_nop 0\n" \
    "v_mul_f32 v66, v59, v61\n" \
    "v_mul_f32 v250, v60, v61\n" \
    "v_pk_mul_f32 v[62:63], v[62:63], v[66:67] op_sel_hi:[1,0]\n" \
    "v_pk_mul_f32 v[64:65], v[64:65], v[66:67] op_sel_hi:[1,0]\n" \
    "v_cvt_pk_bf16_f32 v148, v62, v63\n" \
    "v_cvt_pk_bf16_f32 v149, v64, v65\n" \
    "v_mov_b32 v54, v250\n" \
    "v_mov_b32_dpp v150, v148 row_ror:8 row_mask:0xf bank_mask:0xf bound_ctrl:1\n" \
    "v_mov_b32_dpp v151, v149 row_ror:8 row_mask:0xf bank_mask:0xf bound_ctrl:1\n" \
    "v_permlane16_swap_b32 v250, v54\n" \
    "s_nop 0\n" \
    "v_mfma_f32_16x16x32_bf16 v[62:65], v[144:147], v[148:151], 0\n" \
    "v_mov_b32 v58, v250\n" \
    "v_mov_b32 v56, v54\n" \
    "s_nop 0\n" \
    "v_permlane32_swap_b32 v250, v58\n" \
    "v_permlane32_swap_b32 v54, v56\n" \
    "v_pk_mul_f32 v[138:139], v[118:119], v[250:251] op_sel_hi:[1,0]\n" \
    "v_pk_mul_f32 v[140:141], v[120:121], v[250:251] op_sel_hi:[1,0]\n" \
    "v_pk_fma_f32 v[138:139], v[126:127], v[58:59], v[138:139] op_sel_hi:[1,0,1]\n" \
    "v_pk_fma_f32 v[140:141], v[128:129], v[58:59], v[140:141] op_sel_hi:[1,0,1]\n" \
    "v_pk_fma_f32 v[138:139], v[122:123], v[54:55], v[138:139] op_sel_hi:[1,0,1]\n" \
    "v_pk_fma_f32 v[140:141], v[124:125], v[54:55], v[140:141] op_sel_hi:[1,0,1]\n" \
    "v_pk_fma_f32 v[138:139], v[130:131], v[56:57], v[138:139] op_sel_hi:[1,0,1]\n" \
    "v_pk_fma_f32 v[140:141], v[132:133], v[56:57], v[140:141] op_sel_hi:[1,0,1]\n" \
    "v_pk_add_f32 v[138:139], v[138:139], v[62:63]\n" \
    "v_pk_add_f32 v[140:141], v[140:141], v[64:65]\n" \
    "s_mov_b64 exec, s[74:75]\n" \
    "ds_write_b128 v14, v[138:141]\n" \
    "s_mov_b64 exec, -1\n" \
    "LskipW0_%=:\n" \
    "s_waitcnt lgkmcnt(0)\n" \
    "s_barrier\n" \
    "s_add_u32 s60, s60, 1\n" \
    "s_mov_b32 s61, s62\n" \
    "s_add_u32 s67, s60, 1\n" \
    "s_lshr_b32 s68, s67, 5\n" \
    "s_lshl_b32 s68, s68, 4\n" \
    "s_lshr_b32 s68, s72, s68\n" \
    "s_and_b32 s68, s68, 0x2000\n" \
    "s_lshl_b32 s67, s67, 16\n" \
    "s_add_u32 s62, s67, s68\n" \
    "s_add_u32 s63, s63, 458752\n" \
    "s_add_u32 s64, s64, 131072\n" \
    "s_add_u32 s65, s65, 114688\n" \
    "s_cmp_ge_u32 s60, s70\n" \
    "s_cbranch_scc1 LendW0_%=\n" \
    "v_readfirstlane_b32 s67, v254\n" \
    "s_nop 1\n" \
    "s_cmp_lt_u32 s67, 160\n" \
    "s_cbranch_scc1 LslowTW0_%=\n" \
    "LbackTW0_%=:\n" \
    "s_mov_b32 s67, s66\n" \
    "s_cmp_eq_u32 s67, 0\n" \
    "s_cbranch_scc1 LdAW0_%=_e\n" \
    "LdAW0_%=:\n" \
    "s_sub_u32 s67, s67, 1\n" \
    "s_cmp_lg_u32 s67, 0\n" \
    "s_cbranch_scc1 LdAW0_%=\n" \
    "LdAW0_%=_e:\n" \
    "s_branch LloopW0_%=\n" \
    "LslowMW0_%=:\n" \
    "s_mov_b32 s87, 0\n" \
    "buffer_load_dwordx4 v[26:29], v3, s[36:39], s61 offen sc1\n" \
    "buffer_load_dwordx4 v[30:33], v4, s[36:39], s61 offen sc1\n" \
    "s_nop 15\n" \
    "s_nop 15\n" \
    "s_nop 7\n" \
    "LslowMlW0_%=:\n" \
    "s_add_u32 s84, s84, 1\n" \
    "buffer_load_dwordx4 v[26:29], v3, s[36:39], s61 offen sc1\n" \
    "buffer_load_dwordx4 v[30:33], v4, s[36:39], s61 offen sc1\n" \
    "s_waitcnt vmcnt(2)\n" \
    "v_max3_u32 v248, v26, v27, v28\n" \
    "v_max3_u32 v249, v29, v30, v31\n" \
    "v_max3_u32 v248, v248, v32, v33\n" \
    "v_max_u32 v248, v248, v249\n" \
    "v_cmp_eq_u32 vcc, -1, v248\n" \
    "s_cbranch_vccz LokMW0_%=\n" \
    "s_add_u32 s87, s87, 1\n" \
    "s_cmp_lt_u32 s87, 0x8000\n" \
    "s_cbranch_scc1 LslowMlW0_%=\n" \
    "s_branch LokMW0_%=\n" \
    "LslowEW0_%=:\n" \
    "s_mov_b32 s87, 0\n" \
    "buffer_load_dwordx4 v[102:105], v11, s[36:39], s61 offen sc1\n" \
    "buffer_load_dwordx4 v[110:113], v11, s[36:39], s61 offen offset:1024 sc1\n" \
    "buffer_load_dwordx2 v[142:143], v12, s[36:39], s61 offen sc1\n" \
    "s_nop 15\n" \
    "s_nop 15\n" \
    "s_nop 7\n" \
    "LslowElW0_%=:\n" \
    "s_add_u32 s84, s84, 1024\n" \
    "buffer_load_dwordx4 v[102:105], v11, s[36:39], s61 offen sc1\n" \
    "buffer_load_dwordx4 v[110:113], v11, s[36:39], s61 offen offset:1024 sc1\n" \
    "buffer_load_dwordx2 v[142:143], v12, s[36:39], s61 offen sc1\n" \
    "s_waitcnt vmcnt(3)\n" \
    "v_max3_u32 v248, v102, v103, v104\n" \
    "v_max3_u32 v249, v105, v110, v111\n" \
    "v_max3_u32 v248, v248, v112, v113\n" \
    "v_max3_u32 v249, v249, v142, v143\n" \
    "v_max_u32 v248, v248, v249\n" \
    "v_cmp_eq_u32 vcc, -1, v248\n" \
    "s_cbranch_vccz LokEW0_%=\n" \
    "s_add_u32 s87, s87, 1\n" \
    "s_cmp_lt_u32 s87, 0x8000\n" \
    "s_cbranch_scc1 LslowElW0_%=\n" \
    "s_branch LokEW0_%=\n" \
    "LslowPW0_%=:\n" \
    "s_mov_b32 s87, 0\n" \
    "buffer_load_dwordx4 v[134:137], v13, s[36:39], s61 offen sc1\n" \
    "s_nop 15\n" \
    "s_nop 15\n" \
    "s_nop 7\n" \
    "LslowPlW0_%=:\n" \
    "s_add_u32 s84, s84, 1048576\n" \
    "buffer_load_dwordx4 v[134:137], v13, s[36:39], s61 offen sc1\n" \
    "s_waitcnt vmcnt(1)\n" \
    "v_max3_u32 v248, v134, v135, v136\n" \
    "v_max_u32 v248, v248, v137\n" \
    "v_cmp_eq_u32 vcc, -1, v248\n" \
    "s_cbranch_vccz LokPW0_%=\n" \
    "s_add_u32 s87, s87, 1\n" \
    "s_cmp_lt_u32 s87, 0x8000\n" \
    "s_cbranch_scc1 LslowPlW0_%=\n" \
    "s_branch LokPW0_%=\n" \
    "LslowTW0_%=:\n" \
    "s_mov_b32 s87, 0\n" \
    "s_lshr_b32 s68, s60, 2\n" \
    "s_lshl_b32 s68, s68, 8\n" \
    "v_mov_b32 v253, s68\n" \
    "LslowTlW0_%=:\n" \
    "global_load_dword v254, v253, s[40:41] sc1\n" \
    "s_waitcnt vmcnt(0)\n" \
    "v_readfirstlane_b32 s67, v254\n" \
    "s_nop 1\n" \
    "s_cmp_ge_u32 s67, 160\n" \
    "s_cbranch_scc1 LbackTW0_%=\n" \
    "s_sleep 2\n" \
    "s_add_u32 s87, s87, 1\n" \
    "s_cmp_lt_u32 s87, 0x40000\n" \
    "s_cbranch_scc1 LslowTlW0_%=\n" \
    "s_branch LbackTW0_%=\n" \
    "LendW0_%=:\n" \
    "s_waitcnt vmcnt(0)\n" \
    "s_barrier\n" \
    "s_branch Ldone_%=\n" \
    "LwW1_%=:\n" \
    "v_add_u32 v248, 28672, v248\n" \
    "ds_read_b32 v0, v248\n" \
    "ds_read_b32 v1, v248 offset:256\n" \
    "ds_read_b32 v2, v248 offset:512\n" \
    "ds_read_b32 v3, v248 offset:768\n" \
    "ds_read_b32 v4, v248 offset:1024\n" \
    "ds_read_b32 v5, v248 offset:1280\n" \
    "ds_read_b32 v6, v248 offset:1536\n" \
    "ds_read_b32 v7, v248 offset:1792\n" \
    "s_waitcnt lgkmcnt(0)\n" \
    "ds_read_b32 v8, v248 offset:2048\n" \
    "ds_read_b32 v9, v248 offset:2304\n" \
    "ds_read_b32 v10, v248 offset:2560\n" \
    "ds_read_b32 v11, v248 offset:2816\n" \
    "ds_read_b32 v12, v248 offset:3072\n" \
    "ds_read_b32 v13, v248 offset:3328\n" \
    "ds_read_b32 v14, v248 offset:3584\n" \
    "ds_read_b32 v15, v248 offset:3840\n" \
    "s_waitcnt lgkmcnt(0)\n" \
    "ds_read_b32 v16, v248 offset:4096\n" \
    "ds_read_b32 v17, v248 offset:4352\n" \
    "ds_read_b32 v18, v248 offset:4608\n" \
    "ds_read_b32 v19, v248 offset:4864\n" \
    "ds_read_b32 v20, v248 offset:5120\n" \
    "ds_read_b32 v21, v248 offset:5376\n" \
    "ds_read_b32 v22, v248 offset:5632\n" \
    "ds_read_b32 v23, v248 offset:5888\n" \
    "s_waitcnt lgkmcnt(0)\n" \
    "ds_read_b32 v24, v248 offset:6144\n" \
    "s_waitcnt lgkmcnt(0)\n" \
    "s_add_u32 s67, s85, 58368\n" \
    "buffer_load_dwordx4 a[0:3], v21, s[88:91], s67 offen\n" \
    "s_add_u32 s67, s85, 87040\n" \
    "buffer_load_dwordx4 a[4:7], v21, s[88:91], s67 offen\n" \
    "s_add_u32 s67, s85, 287744\n" \
    "buffer_load_dwordx4 a[8:11], v21, s[88:91], s67 offen\n" \
    "s_add_u32 s67, s85, 316416\n" \
    "buffer_load_dwordx4 a[12:15], v21, s[88:91], s67 offen\n" \
    "s_add_u32 s67, s85, 1024\n" \
    "buffer_load_dwordx4 a[16:19], v21, s[88:91], s67 offen\n" \
    "s_add_u32 s67, s85, 29696\n" \
    "buffer_load_dwordx4 a[20:23], v21, s[88:91], s67 offen\n" \
    "s_add_u32 s67, s85, 115712\n" \
    "buffer_load_dwordx4 a[24:27], v21, s[88:91], s67 offen\n" \
    "s_add_u32 s67, s85, 144384\n" \
    "buffer_load_dwordx4 a[28:31], v21, s[88:91], s67 offen\n" \
    "s_add_u32 s67, s85, 173056\n" \
    "buffer_load_dwordx4 a[32:35], v21, s[88:91], s67 offen\n" \
    "s_add_u32 s67, s85, 201728\n" \
    "buffer_load_dwordx4 a[36:39], v21, s[88:91], s67 offen\n" \
    "s_add_u32 s67, s85, 230400\n" \
    "buffer_load_dwordx4 a[40:43], v21, s[88:91], s67 offen\n" \
    "s_add_u32 s67, s85, 259072\n" \
    "buffer_load_dwordx4 a[44:47], v21, s[88:91], s67 offen\n" \
    "s_add_u32 s67, s85, 345088\n" \
    "buffer_load_dwordx4 a[48:51], v21, s[88:91], s67 offen\n" \
    "s_add_u32 s67, s85, 373760\n" \
    "buffer_load_dwordx4 a[52:55], v21, s[88:91], s67 offen\n" \
    "s_add_u32 s67, s85, 59392\n" \
    "buffer_load_dwordx4 a[56:59], v20, s[88:91], s67 offen\n" \
    "s_add_u32 s67, s85, 88064\n" \
    "buffer_load_dwordx4 a[60:63], v20, s[88:91], s67 offen\n" \
    "s_add_u32 s67, s85, 288768\n" \
    "buffer_load_dwordx4 a[64:67], v20, s[88:91], s67 offen\n" \
    "s_add_u32 s67, s85, 317440\n" \
    "buffer_load_dwordx4 a[68:71], v20, s[88:91], s67 offen\n" \
    "s_add_u32 s67, s85, 2048\n" \
    "buffer_load_dwordx4 a[72:75], v20, s[88:91], s67 offen\n" \
    "s_add_u32 s67, s85, 30720\n" \
    "buffer_load_dwordx4 a[76:79], v20, s[88:91], s67 offen\n" \
    "s_add_u32 s67, s85, 116736\n" \
    "buffer_load_dwordx4 a[80:83], v20, s[88:91], s67 offen\n" \
    "s_add_u32 s67, s85, 145408\n" \
    "buffer_load_dwordx4 a[84:87], v20, s[88:91], s67 offen\n" \
    "s_add_u32 s67, s85, 174080\n" \
    "buffer_load_dwordx4 a[88:91], v20, s[88:91], s67 offen\n" \
    "s_add_u32 s67, s85, 202752\n" \
    "buffer_load_dwordx4 a[92:95], v20, s[88:91], s67 offen\n" \
    "s_add_u32 s67, s85, 231424\n" \
    "buffer_load_dwordx4 a[96:99], v20, s[88:91], s67 offen\n" \
    "s_add_u32 s67, s85, 260096\n" \
    "buffer_load_dwordx4 a[100:103], v20, s[88:91], s67 offen\n" \
    "s_add_u32 s67, s85, 346112\n" \
    "buffer_load_dwordx4 a[104:107], v20, s[88:91], s67 offen\n" \
    "s_add_u32 s67, s85, 374784\n" \
    "buffer_load_dwordx4 a[108:111], v20, s[88:91], s67 offen\n" \
    "s_add_u32 s67, s85, 59392\n" \
    "buffer_load_dwordx4 a[112:115], v21, s[88:91], s67 offen\n" \
    "s_add_u32 s67, s85, 88064\n" \
    "buffer_load_dwordx4 a[116:119], v21, s[88:91], s67 offen\n" \
    "s_add_u32 s67, s85, 288768\n" \
    "buffer_load_dwordx4 a[120:123], v21, s[88:91], s67 offen\n" \
    "s_add_u32 s67, s85, 317440\n" \
    "buffer_load_dwordx4 a[124:127], v21, s[88:91], s67 offen\n" \
    "s_add_u32 s67, s85, 2048\n" \
    "buffer_load_dwordx4 a[128:131], v21, s[88:91], s67 offen\n" \
    "s_add_u32 s67, s85, 30720\n" \
    "buffer_load_dwordx4 a[132:135], v21, s[88:91], s67 offen\n" \
    "s_add_u32 s67, s85, 116736\n" \
    "buffer_load_dwordx4 a[136:139], v21, s[88:91], s67 offen\n" \
    "s_add_u32 s67, s85, 145408\n" \
    "buffer_load_dwordx4 a[140:143], v21, s[88:91], s67 offen\n" \
    "s_add_u32 s67, s85, 174080\n" \
    "buffer_load_dwordx4 a[144:147], v21, s[88:91], s67 offen\n" \
    "s_add_u32 s67, s85, 202752\n" \
    "buffer_load_dwordx4 a[148:151], v21, s[88:91], s67 offen\n" \
    "s_add_u32 s67, s85, 231424\n" \
    "buffer_load_dwordx4 a[152:155], v21, s[88:91], s67 offen\n" \
    "s_add_u32 s67, s85, 260096\n" \
    "buffer_load_dwordx4 a[156:159], v21, s[88:91], s67 offen\n" \
    "s_add_u32 s67, s85, 346112\n" \
    "buffer_load_dwordx4 a[160:163], v21, s[88:91], s67 offen\n" \
    "s_add_u32 s67, s85, 374784\n" \
    "buffer_load_dwordx4 a[164:167], v21, s[88:91], s67 offen\n" \
    "s_add_u32 s67, s85, 63488\n" \
    "buffer_load_dwordx4 a[168:171], v21, s[88:91], s67 offen\n" \
    "s_add_u32 s67, s85, 92160\n" \
    "buffer_load_dwordx4 a[172:175], v21, s[88:91], s67 offen\n" \
    "s_add_u32 s67, s85, 6144\n" \
    "buffer_load_dwordx4 a[176:179], v21, s[88:91], s67 offen\n" \
    "s_add_u32 s67, s85, 34816\n" \
    "buffer_load_dwordx4 a[180:183], v21, s[88:91], s67 offen\n" \
    "s_add_u32 s67, s85, 120832\n" \
    "buffer_load_dwordx4 a[184:187], v21, s[88:91], s67 offen\n" \
    "s_add_u32 s67, s85, 149504\n" \
    "buffer_load_dwordx4 a[188:191], v21, s[88:91], s67 offen\n" \
    "s_add_u32 s67, s85, 178176\n" \
    "buffer_load_dwordx4 a[192:195], v21, s[88:91], s67 offen\n" \
    "s_add_u32 s67, s85, 206848\n" \
    "buffer_load_dwordx4 a[196:199], v21, s[88:91], s67 offen\n" \
    "s_waitcnt vmcnt(0)\n" \
    "s_add_u32 s67, s86, 1662976\n" \
    "buffer_load_dwordx4 a[200:203], v22, s[92:95], s67 offen\n" \
    "s_add_u32 s67, s86, 1691648\n" \
    "buffer_load_dwordx4 a[204:207], v22, s[92:95], s67 offen\n" \
    "s_add_u32 s67, s86, 1892352\n" \
    "buffer_load_dwordx4 a[208:211], v22, s[92:95], s67 offen\n" \
    "s_add_u32 s67, s86, 1921024\n" \
    "buffer_load_dwordx4 a[212:215], v22, s[92:95], s67 offen\n" \
    "s_add_u32 s67, s86, 1605632\n" \
    "buffer_load_dwordx4 a[216:219], v22, s[92:95], s67 offen\n" \
    "s_add_u32 s67, s86, 1634304\n" \
    "buffer_load_dwordx4 a[220:223], v22, s[92:95], s67 offen\n" \
    "s_add_u32 s67, s86, 1720320\n" \
    "buffer_load_dwordx4 a[224:227], v22, s[92:95], s67 offen\n" \
    "s_add_u32 s67, s86, 1748992\n" \
    "buffer_load_dwordx4 a[228:231], v22, s[92:95], s67 offen\n" \
    "s_add_u32 s67, s86, 1777664\n" \
    "buffer_load_dwordx4 a[232:235], v22, s[92:95], s67 offen\n" \
    "s_add_u32 s67, s86, 1806336\n" \
    "buffer_load_dwordx4 a[236:239], v22, s[92:95], s67 offen\n" \
    "s_add_u32 s67, s86, 1835008\n" \
    "buffer_load_dwordx4 a[240:243], v22, s[92:95], s67 offen\n" \
    "s_add_u32 s67, s86, 1863680\n" \
    "buffer_load_dwordx4 a[244:247], v22, s[92:95], s67 offen\n" \
    "s_add_u32 s67, s86, 1949696\n" \
    "buffer_load_dwordx4 a[248:251], v22, s[92:95], s67 offen\n" \
    "s_add_u32 s67, s86, 1978368\n" \
    "buffer_load_dwordx4 a[252:255], v22, s[92:95], s67 offen\n" \
    "s_add_u32 s67, s86, 2064384\n" \
    "buffer_load_dwordx4 v[192:195], v22, s[92:95], s67 offen\n" \
    "s_add_u32 s67, s86, 2093056\n" \
    "buffer_load_dwordx4 v[196:199], v22, s[92:95], s67 offen\n" \
    "s_add_u32 s67, s86, 2293760\n" \
    "buffer_load_dwordx4 v[200:203], v22, s[92:95], s67 offen\n" \
    "s_add_u32 s67, s86, 2322432\n" \
    "buffer_load_dwordx4 v[204:207], v22, s[92:95], s67 offen\n" \
    "s_add_u32 s67, s86, 2007040\n" \
    "buffer_load_dwordx4 v[208:211], v22, s[92:95], s67 offen\n" \
    "s_add_u32 s67, s86, 2035712\n" \
    "buffer_load_dwordx4 v[212:215], v22, s[92:95], s67 offen\n" \
    "s_add_u32 s67, s86, 2121728\n" \
    "buffer_load_dwordx4 v[216:219], v22, s[92:95], s67 offen\n" \
    "s_add_u32 s67, s86, 2150400\n" \
    "buffer_load_dwordx4 v[220:223], v22, s[92:95], s67 offen\n" \
    "s_add_u32 s67, s86, 2179072\n" \
    "buffer_load_dwordx4 v[224:227], v22, s[92:95], s67 offen\n" \
    "s_add_u32 s67, s86, 2207744\n" \
    "buffer_load_dwordx4 v[228:231], v22, s[92:95], s67 offen\n" \
    "s_add_u32 s67, s86, 2236416\n" \
    "buffer_load_dwordx4 v[232:235], v22, s[92:95], s67 offen\n" \
    "s_add_u32 s67, s86, 2265088\n" \
    "buffer_load_dwordx4 v[236:239], v22, s[92:95], s67 offen\n" \
    "s_add_u32 s67, s86, 2351104\n" \
    "buffer_load_dwordx4 v[240:243], v22, s[92:95], s67 offen\n" \
    "s_add_u32 s67, s86, 2379776\n" \
    "buffer_load_dwordx4 v[244:247], v22, s[92:95], s67 offen\n" \
    "s_add_u32 s67, s86, 2465792\n" \
    "buffer_load_dwordx4 v[152:155], v22, s[92:95], s67 offen\n" \
    "s_add_u32 s67, s86, 2494464\n" \
    "buffer_load_dwordx4 v[156:159], v22, s[92:95], s67 offen\n" \
    "s_add_u32 s67, s86, 2695168\n" \
    "buffer_load_dwordx4 v[160:163], v22, s[92:95], s67 offen\n" \
    "s_add_u32 s67, s86, 2723840\n" \
    "buffer_load_dwordx4 v[164:167], v22, s[92:95], s67 offen\n" \
    "s_add_u32 s67, s86, 2408448\n" \
    "buffer_load_dwordx4 v[168:171], v22, s[92:95], s67 offen\n" \
    "s_add_u32 s67, s86, 2437120\n" \
    "buffer_load_dwordx4 v[172:175], v22, s[92:95], s67 offen\n" \
    "s_add_u32 s67, s86, 2523136\n" \
    "buffer_load_dwordx4 v[176:179], v22, s[92:95], s67 offen\n" \
    "s_add_u32 s67, s86, 2551808\n" \
    "buffer_load_dwordx4 v[180:183], v22, s[92:95], s67 offen\n" \
    "s_add_u32 s67, s86, 2580480\n" \
    "buffer_load_dwordx4 v[184:187], v22, s[92:95], s67 offen\n" \
    "s_add_u32 s67, s86, 2609152\n" \
    "buffer_load_dwordx4 v[188:191], v22, s[92:95], s67 offen\n" \
    "s_waitcnt vmcnt(0)\n" \
    "ds_write_b128 v17, v[152:155]\n" \
    "ds_write_b128 v17, v[156:159] offset:1024\n" \
    "ds_write_b128 v17, v[160:163] offset:2048\n" \
    "ds_write_b128 v17, v[164:167] offset:3072\n" \
    "ds_write_b128 v17, v[168:171] offset:4096\n" \
    "ds_write_b128 v17, v[172:175] offset:5120\n" \
    "ds_write_b128 v17, v[176:179] offset:6144\n" \
    "ds_write_b128 v17, v[180:183] offset:7168\n" \
    "ds_write_b128 v17, v[184:187] offset:8192\n" \
    "ds_write_b128 v17, v[188:191] offset:9216\n" \
    "s_waitcnt lgkmcnt(0)\n" \
    "s_add_u32 s67, s86, 2637824\n" \
    "buffer_load_dwordx4 v[152:155], v22, s[92:95], s67 offen\n" \
    "s_add_u32 s67, s86, 2666496\n" \
    "buffer_load_dwordx4 v[156:159], v22, s[92:95], s67 offen\n" \
    "s_add_u32 s67, s86, 2752512\n" \
    "buffer_load_dwordx4 v[160:163], v22, s[92:95], s67 offen\n" \
    "s_add_u32 s67, s86, 2781184\n" \
    "buffer_load_dwordx4 v[164:167], v22, s[92:95], s67 offen\n" \
    "s_add_u32 s67, s86, 2867200\n" \
    "buffer_load_dwordx4 v[168:171], v22, s[92:95], s67 offen\n" \
    "s_add_u32 s67, s86, 2895872\n" \
    "buffer_load_dwordx4 v[172:175], v22, s[92:95], s67 offen\n" \
    "s_add_u32 s67, s86, 3096576\n" \
    "buffer_load_dwordx4 v[176:179], v22, s[92:95], s67 offen\n" \
    "s_add_u32 s67, s86, 3125248\n" \
    "buffer_load_dwordx4 v[180:183], v22, s[92:95], s67 offen\n" \
    "s_add_u32 s67, s86, 2809856\n" \
    "buffer_load_dwordx4 v[184:187], v22, s[92:95], s67 offen\n" \
    "s_add_u32 s67, s86, 2838528\n" \
    "buffer_load_dwordx4 v[188:191], v22, s[92:95], s67 offen\n" \
    "s_waitcnt vmcnt(0)\n" \
    "ds_write_b128 v17, v[152:155] offset:10240\n" \
    "ds_write_b128 v17, v[156:159] offset:11264\n" \
    "ds_write_b128 v17, v[160:163] offset:12288\n" \
    "ds_write_b128 v17, v[164:167] offset:13312\n" \
    "ds_write_b128 v17, v[168:171] offset:14336\n" \
    "ds_write_b128 v17, v[172:175] offset:15360\n" \
    "ds_write_b128 v17, v[176:179] offset:16384\n" \
    "ds_write_b128 v17, v[180:183] offset:17408\n" \
    "ds_write_b128 v17, v[184:187] offset:18432\n" \
    "ds_write_b128 v17, v[188:191] offset:19456\n" \
    "s_waitcnt lgkmcnt(0)\n" \
    "s_add_u32 s67, s86, 2924544\n" \
    "buffer_load_dwordx4 v[152:155], v22, s[92:95], s67 offen\n" \
    "s_add_u32 s67, s86, 2953216\n" \
    "buffer_load_dwordx4 v[156:159], v22, s[92:95], s67 offen\n" \
    "s_add_u32 s67, s86, 2981888\n" \
    "buffer_load_dwordx4 v[160:163], v22, s[92:95], s67 offen\n" \
    "s_add_u32 s67, s86, 3010560\n" \
    "buffer_load_dwordx4 v[164:167], v22, s[92:95], s67 offen\n" \
    "s_add_u32 s67, s86, 3039232\n" \
    "buffer_load_dwordx4 v[168:171], v22, s[92:95], s67 offen\n" \
    "s_add_u32 s67, s86, 3067904\n" \
    "buffer_load_dwordx4 v[172:175], v22, s[92:95], s67 offen\n" \
    "s_add_u32 s67, s86, 3153920\n" \
    "buffer_load_dwordx4 v[176:179], v22, s[92:95], s67 offen\n" \
    "s_add_u32 s67, s86, 3182592\n" \
    "buffer_load_dwordx4 v[180:183], v22, s[92:95], s67 offen\n" \
    "s_waitcnt vmcnt(0)\n" \
    "ds_write_b128 v17, v[152:155] offset:20480\n" \
    "ds_write_b128 v17, v[156:159] offset:21504\n" \
    "ds_write_b128 v17, v[160:163] offset:22528\n" \
    "ds_write_b128 v17, v[164:167] offset:23552\n" \
    "ds_write_b128 v17, v[168:171] offset:24576\n" \
    "ds_write_b128 v17, v[172:175] offset:25600\n" \
    "ds_write_b128 v17, v[176:179] offset:26624\n" \
    "ds_write_b128 v17, v[180:183] offset:27648\n" \
    "s_waitcnt lgkmcnt(0)\n" \
    "s_mov_b32 s87, 0\n" \
    "Lt0W1_%=:\n" \
    "v_mov_b32 v248, 0\n" \
    "global_load_dword v254, v248, s[40:41] sc1\n" \
    "s_waitcnt vmcnt(0)\n" \
    "v_readfirstlane_b32 s67, v254\n" \
    "s_nop 1\n" \
    "s_cmp_ge_u32 s67, 160\n" \
    "s_cbranch_scc1 Lt0dW1_%=\n" \
    "s_sleep 4\n" \
    "s_add_u32 s87, s87, 1\n" \
    "s_cmp_lt_u32 s87, 0x40000\n" \
    "s_cbranch_scc1 Lt0W1_%=\n" \
    "Lt0dW1_%=:\n" \
    "s_mov_b32 s87, 0\n" \
    "Lp0W1_%=:\n" \
    "v_mov_b32 v248, 45120\n" \
    "global_load_dword v254, v248, s[40:41] sc1\n" \
    "s_waitcnt vmcnt(0)\n" \
    "v_readfirstlane_b32 s72, v254\n" \
    "s_nop 1\n" \
    "s_cmp_lg_u32 s72, -1\n" \
    "s_cbranch_scc1 Lp0dW1_%=\n" \
    "s_add_u32 s87, s87, 1\n" \
    "s_cmp_lt_u32 s87, 0x100000\n" \
    "s_cbranch_scc1 Lp0W1_%=\n" \
    "s_mov_b32 s72, 0\n" \
    "Lp0dW1_%=:\n" \
    "s_and_b32 s61, s72, 0x2000\n" \
    "s_add_u32 s62, s61, 65536\n" \
    "s_waitcnt vmcnt(0)\n" \
    "s_waitcnt lgkmcnt(0)\n" \
    "s_barrier\n" \
    "s_memrealtime s[96:97]\n" \
    "s_waitcnt lgkmcnt(0)\n" \
    "LloopW1_%=:\n" \
    "buffer_load_dwordx4 v[26:29], v3, s[36:39], s61 offen sc1\n" \
    "buffer_load_dwordx4 v[30:33], v4, s[36:39], s61 offen sc1\n" \
    "v_mov_b32 v54, 0\n" \
    "v_mov_b32 v55, 0\n" \
    "v_mov_b32 v56, 0\n" \
    "v_mov_b32 v57, 0\n" \
    "v_mov_b32 v58, 0\n" \
    "v_mov_b32 v59, 0\n" \
    "v_mov_b32 v60, 0\n" \
    "v_mov_b32 v61, 0\n" \
    "v_mov_b32 v62, 0\n" \
    "v_mov_b32 v63, 0\n" \
    "v_mov_b32 v64, 0\n" \
    "v_mov_b32 v65, 0\n" \
    "v_mov_b32 v66, 0\n" \
    "v_mov_b32 v67, 0\n" \
    "v_mov_b32 v68, 0\n" \
    "v_mov_b32 v69, 0\n" \
    "v_mov_b32 v70, 0\n" \
    "v_mov_b32 v71, 0\n" \
    "v_mov_b32 v72, 0\n" \
    "v_mov_b32 v73, 0\n" \
    "v_mov_b32 v74, 0\n" \
    "v_mov_b32 v75, 0\n" \
    "v_mov_b32 v76, 0\n" \
    "v_mov_b32 v77, 0\n" \
    "v_mov_b32 v78, 0\n" \
    "v_mov_b32 v79, 0\n" \
    "v_mov_b32 v80, 0\n" \
    "v_mov_b32 v81, 0\n" \
    "v_mov_b32 v82, 0\n" \
    "v_mov_b32 v83, 0\n" \
    "v_mov_b32 v84, 0\n" \
    "v_mov_b32 v85, 0\n" \
    "s_nop 15\n" \
    "s_nop 7\n" \
    "buffer_load_dwordx4 v[26:29], v3, s[36:39], s61 offen sc1\n" \
    "buffer_load_dwordx4 v[30:33], v4, s[36:39], s61 offen sc1\n" \
    "s_waitcnt vmcnt(2)\n" \
    "v_max3_u32 v248, v26, v27, v28\n" \
    "v_max3_u32 v249, v29, v30, v31\n" \
    "v_max3_u32 v248, v248, v32, v33\n" \
    "v_max_u32 v248, v248, v249\n" \
    "v_cmp_eq_u32 vcc, -1, v248\n" \
    "s_cbranch_vccz LokMW1_%=\n" \
    "s_add_u32 s84, s84, 1\n" \
    "s_waitcnt vmcnt(0)\n" \
    "v_max3_u32 v248, v26, v27, v28\n" \
    "v_max3_u32 v249, v29, v30, v31\n" \
    "v_max3_u32 v248, v248, v32, v33\n" \
    "v_max_u32 v248, v248, v249\n" \
    "v_cmp_eq_u32 vcc, -1, v248\n" \
    "s_cbranch_vccz LokMW1_%=\n" \
    "s_add_u32 s84, s84, 1\n" \
    "s_branch LslowMW1_%=\n" \
    "LokMW1_%=:\n" \
    "s_nop 0\n" \
    "ds_write_b128 v5, v[26:29] offset:1024\n" \
    "ds_write_b128 v5, v[30:33] offset:5120\n" \
    "v_smfmac_f32_16x16x64_bf16 v[54:57], v[26:29], a[0:7], v1\n" \
    "v_smfmac_f32_16x16x64_bf16 v[58:61], v[26:29], a[56:63], v1\n" \
    "v_smfmac_f32_16x16x64_bf16 v[62:65], v[26:29], a[112:119], v1\n" \
    "v_smfmac_f32_16x16x64_bf16 v[66:69], v[26:29], a[168:175], v1\n" \
    "s_waitcnt lgkmcnt(0)\n" \
    "s_barrier\n" \
    "ds_read_b128 v[34:37], v5\n" \
    "ds_read_b128 v[38:41], v5 offset:2048\n" \
    "ds_read_b128 v[42:45], v5 offset:3072\n" \
    "ds_read_b128 v[46:49], v5 offset:4096\n" \
    "ds_read_b128 v[50:53], v5 offset:6144\n" \
    "v_smfmac_f32_16x16x64_bf16 v[54:57], v[30:33], a[8:15], v1\n" \
    "v_smfmac_f32_16x16x64_bf16 v[58:61], v[30:33], a[64:71], v1\n" \
    "v_smfmac_f32_16x16x64_bf16 v[62:65], v[30:33], a[120:127], v1\n" \
    "s_waitcnt lgkmcnt(4)\n" \
    "v_smfmac_f32_16x16x64_bf16 v[54:57], v[34:37], a[16:23], v1\n" \
    "v_smfmac_f32_16x16x64_bf16 v[58:61], v[34:37], a[72:79], v1\n" \
    "v_smfmac_f32_16x16x64_bf16 v[62:65], v[34:37], a[128:135], v1\n" \
    "v_smfmac_f32_16x16x64_bf16 v[66:69], v[34:37], a[176:183], v1\n" \
    "buffer_load_dwordx4 v[86:89], v0, s[44:47], s63 offen sc1\n" \
    "s_waitcnt lgkmcnt(3)\n" \
    "v_smfmac_f32_16x16x64_bf16 v[66:69], v[38:41], a[184:191], v1\n" \
    "s_waitcnt lgkmcnt(2)\n" \
    "v_smfmac_f32_16x16x64_bf16 v[66:69], v[42:45], a[192:199], v1\n" \
    "v_smfmac_f32_16x16x64_bf16 v[54:57], v[38:41], a[24:31], v1\n" \
    "ds_read_b128 v[152:155], v17\n" \
    "ds_read_b128 v[156:159], v17 offset:1024\n" \
    "ds_read_b128 v[160:163], v17 offset:2048\n" \
    "v_smfmac_f32_16x16x64_bf16 v[54:57], v[42:45], a[32:39], v1\n" \
    "buffer_load_dwordx4 v[90:93], v0, s[44:47], s63 offen offset:1024 sc1\n" \
    "ds_read_b128 v[164:167], v17 offset:3072\n" \
    "ds_read_b128 v[168:171], v17 offset:4096\n" \
    "ds_read_b128 v[172:175], v17 offset:5120\n" \
    "s_waitcnt lgkmcnt(7)\n" \
    "v_smfmac_f32_16x16x64_bf16 v[54:57], v[46:49], a[40:47], v1\n" \
    "v_add_f32 v250, v66, v67\n" \
    "v_add_f32 v251, v68, v69\n" \
    "v_cvt_pk_bf16_f32 v250, v250, v251\n" \
    "s_waitcnt lgkmcnt(6)\n" \
    "v_smfmac_f32_16x16x64_bf16 v[54:57], v[50:53], a[48:55], v1\n" \
    "ds_write_b32 v7, v250\n" \
    "ds_read_b128 v[176:179], v17 offset:6144\n" \
    "ds_read_b128 v[180:183], v17 offset:7168\n" \
    "v_smfmac_f32_16x16x64_bf16 v[58:61], v[38:41], a[80:87], v1\n" \
    "ds_read_b128 v[184:187], v17 offset:8192\n" \
    "ds_read_b128 v[188:191], v17 offset:9216\n" \
    "v_smfmac_f32_16x16x64_bf16 v[58:61], v[42:45], a[88:95], v1\n" \
    "buffer_load_dwordx4 v[94:97], v0, s[44:47], s63 offen offset:2048 sc1\n" \
    "v_smfmac_f32_16x16x64_bf16 v[58:61], v[46:49], a[96:103], v1\n" \
    "v_add_f32 v248, v54, v55\n" \
    "v_add_f32 v249, v56, v57\n" \
    "v_cvt_pk_bf16_f32 v248, v248, v249\n" \
    "v_smfmac_f32_16x16x64_bf16 v[58:61], v[50:53], a[104:111], v1\n" \
    "ds_write_b32 v6, v248\n" \
    "v_smfmac_f32_16x16x64_bf16 v[62:65], v[38:41], a[136:143], v1\n" \
    "v_smfmac_f32_16x16x64_bf16 v[62:65], v[42:45], a[144:151], v1\n" \
    "buffer_load_dwordx4 v[98:101], v0, s[44:47], s63 offen offset:3072 sc1\n" \
    "v_smfmac_f32_16x16x64_bf16 v[62:65], v[46:49], a[152:159], v1\n" \
    "s_nop 2\n" \
    "v_add_f32 v250, v58, v59\n" \
    "v_add_f32 v251, v60, v61\n" \
    "v_cvt_pk_bf16_f32 v250, v250, v251\n" \
    "v_smfmac_f32_16x16x64_bf16 v[62:65], v[50:53], a[160:167], v1\n" \
    "ds_write_b32 v6, v250 offset:256\n" \
    "s_nop 6\n" \
    "v_add_f32 v248, v62, v63\n" \
    "v_add_f32 v249, v64, v65\n" \
    "v_cvt_pk_bf16_f32 v248, v248, v249\n" \
    "ds_write_b32 v6, v248 offset:512\n" \
    "s_waitcnt lgkmcnt(0)\n" \
    "s_barrier\n" \
    "s_memtime s[76:77]\n" \
    "s_waitcnt lgkmcnt(0)\n" \
    "ds_read_b64_tr_b16 v[118:119], v18\n" \
    "ds_read_b64_tr_b16 v[120:121], v18 offset:64\n" \
    "v_smfmac_f32_16x16x64_bf16 v[70:73], v[26:29], a[200:207], v1\n" \
    "v_smfmac_f32_16x16x64_bf16 v[78:81], v[26:29], v[152:159], v1\n" \
    "ds_read_b128 v[152:155], v17 offset:10240\n" \
    "ds_read_b128 v[156:159], v17 offset:11264\n" \
    "v_smfmac_f32_16x16x64_bf16 v[70:73], v[30:33], a[208:215], v1\n" \
    "v_smfmac_f32_16x16x64_bf16 v[78:81], v[30:33], v[160:167], v1\n" \
    "ds_read_b128 v[160:163], v17 offset:12288\n" \
    "ds_read_b128 v[164:167], v17 offset:13312\n" \
    "v_smfmac_f32_16x16x64_bf16 v[70:73], v[34:37], a[216:223], v1\n" \
    "v_smfmac_f32_16x16x64_bf16 v[78:81], v[34:37], v[168:175], v1\n" \
    "ds_read_b128 v[168:171], v17 offset:14336\n" \
    "ds_read_b128 v[172:175], v17 offset:15360\n" \
    "v_smfmac_f32_16x16x64_bf16 v[70:73], v[38:41], a[224:231], v1\n" \
    "v_smfmac_f32_16x16x64_bf16 v[78:81], v[38:41], v[176:183], v1\n" \
    "ds_read_b128 v[176:179], v17 offset:16384\n" \
    "ds_read_b128 v[180:183], v17 offset:17408\n" \
    "v_smfmac_f32_16x16x64_bf16 v[70:73], v[42:45], a[232:239], v1\n" \
    "v_smfmac_f32_16x16x64_bf16 v[78:81], v[42:45], v[184:191], v1\n" \
    "ds_read_b128 v[184:187], v17 offset:18432\n" \
    "ds_read_b128 v[188:191], v17 offset:19456\n" \
    "v_smfmac_f32_16x16x64_bf16 v[70:73], v[46:49], a[240:247], v1\n" \
    "s_waitcnt lgkmcnt(8)\n" \
    "v_smfmac_f32_16x16x64_bf16 v[78:81], v[46:49], v[152:159], v1\n" \
    "ds_read_b128 v[152:155], v17 offset:20480\n" \
    "ds_read_b128 v[156:159], v17 offset:21504\n" \
    "s_add_u32 s67, s60, 1\n" \
    "s_lshr_b32 s67, s67, 2\n" \
    "s_min_u32 s67, s67, 15\n" \
    "s_lshl_b32 s67, s67, 8\n" \
    "v_mov_b32 v253, s67\n" \
    "global_load_dword v254, v253, s[40:41] sc1\n" \
    "v_smfmac_f32_16x16x64_bf16 v[70:73], v[50:53], a[248:255], v1\n" \
    "s_waitcnt lgkmcnt(8)\n" \
    "v_smfmac_f32_16x16x64_bf16 v[78:81], v[50:53], v[160:167], v1\n" \
    "ds_read_b128 v[160:163], v17 offset:22528\n" \
    "ds_read_b128 v[164:167], v17 offset:23552\n" \
    "v_smfmac_f32_16x16x64_bf16 v[74:77], v[26:29], v[192:199], v1\n" \
    "s_waitcnt lgkmcnt(8)\n" \
    "v_smfmac_f32_16x16x64_bf16 v[82:85], v[26:29], v[168:175], v1\n" \
    "ds_read_b128 v[168:171], v17 offset:24576\n" \
    "ds_read_b128 v[172:175], v17 offset:25600\n" \
    "v_smfmac_f32_16x16x64_bf16 v[74:77], v[30:33], v[200:207], v1\n" \
    "s_waitcnt lgkmcnt(8)\n" \
    "v_smfmac_f32_16x16x64_bf16 v[82:85], v[30:33], v[176:183], v1\n" \
    "ds_read_b128 v[176:179], v17 offset:26624\n" \
    "ds_read_b128 v[180:183], v17 offset:27648\n" \
    "v_smfmac_f32_16x16x64_bf16 v[74:77], v[34:37], v[208:215], v1\n" \
    "s_waitcnt lgkmcnt(8)\n" \
    "v_smfmac_f32_16x16x64_bf16 v[82:85], v[34:37], v[184:191], v1\n" \
    "v_add_f32 v70, v70, v71\n" \
    "v_add_f32 v72, v72, v73\n" \
    "v_cvt_pk_bf16_f32 v144, v70, v72\n" \
    "v_smfmac_f32_16x16x64_bf16 v[74:77], v[38:41], v[216:223], v1\n" \
    "s_waitcnt lgkmcnt(6)\n" \
    "v_smfmac_f32_16x16x64_bf16 v[82:85], v[38:41], v[152:159], v1\n" \
    "buffer_store_dwordx4 v[118:121], v19, s[48:51], s64 offen sc1\n" \
    "v_add_f32 v78, v78, v79\n" \
    "v_add_f32 v80, v80, v81\n" \
    "v_cvt_pk_bf16_f32 v146, v78, v80\n" \
    "v_smfmac_f32_16x16x64_bf16 v[74:77], v[42:45], v[224:231], v1\n" \
    "s_waitcnt lgkmcnt(4)\n" \
    "v_smfmac_f32_16x16x64_bf16 v[82:85], v[42:45], v[160:167], v1\n" \
    "v_smfmac_f32_16x16x64_bf16 v[74:77], v[46:49], v[232:239], v1\n" \
    "s_waitcnt lgkmcnt(2)\n" \
    "v_smfmac_f32_16x16x64_bf16 v[82:85], v[46:49], v[168:175], v1\n" \
    "v_smfmac_f32_16x16x64_bf16 v[74:77], v[50:53], v[240:247], v1\n" \
    "s_waitcnt lgkmcnt(0)\n" \
    "v_smfmac_f32_16x16x64_bf16 v[82:85], v[50:53], v[176:183], v1\n" \
    "s_nop 5\n" \
    "v_add_f32 v74, v74, v75\n" \
    "v_add_f32 v76, v76, v77\n" \
    "v_cvt_pk_bf16_f32 v145, v74, v76\n" \
    "v_add_f32 v82, v82, v83\n" \
    "v_add_f32 v84, v84, v85\n" \
    "v_cvt_pk_bf16_f32 v147, v82, v84\n" \
    "v_permlane32_swap_b32 v144, v146\n" \
    "s_nop 0\n" \
    "v_permlane32_swap_b32 v145, v147\n" \
    "s_nop 1\n" \
    "v_permlane16_swap_b32 v144, v145\n" \
    "v_permlane16_swap_b32 v146, v147\n" \
    "s_cmp_eq_u32 s69, 0\n" \
    "s_cbranch_scc1 LnoVMW1_%=\n" \
    "buffer_store_dwordx4 v[144:147], v0, s[52:55], s65 offen sc1\n" \
    "LnoVMW1_%=:\n" \
    "s_memtime s[78:79]\n" \
    "s_waitcnt lgkmcnt(0)\n" \
    "s_sub_u32 s67, s78, s76\n" \
    "s_sub_i32 s67, 790, s67\n" \
    "s_cmp_lt_i32 s67, 36\n" \
    "s_cbranch_scc1 Ltg0W1_%=\n" \
    "s_mul_i32 s67, s67, 29\n" \
    "s_lshr_b32 s67, s67, 10\n" \
    "Ltl0W1_%=:\n" \
    "s_sub_u32 s67, s67, 1\n" \
    "s_cmp_lg_u32 s67, 0\n" \
    "s_cbranch_scc1 Ltl0W1_%=\n" \
    "Ltg0W1_%=:\n" \
    "buffer_load_dwordx4 v[134:137], v13, s[36:39], s61 offen sc1\n" \
    "s_nop 15\n" \
    "s_nop 15\n" \
    "buffer_load_dwordx4 v[102:105], v11, s[36:39], s61 offen sc1\n" \
    "buffer_load_dwordx4 v[110:113], v11, s[36:39], s61 offen offset:1024 sc1\n" \
    "buffer_load_dwordx2 v[142:143], v12, s[36:39], s61 offen sc1\n" \
    "s_nop 15\n" \
    "s_nop 15\n" \
    "s_nop 14\n" \
    "buffer_load_dwordx4 v[134:137], v13, s[36:39], s61 offen sc1\n" \
    "s_cmp_eq_u32 s60, 63\n" \
    "s_cbranch_scc1 LskipW1_%=\n" \
    "s_waitcnt vmcnt(4)\n" \
    "v_max3_u32 v248, v134, v135, v136\n" \
    "v_max_u32 v248, v248, v137\n" \
    "v_cmp_eq_u32 vcc, -1, v248\n" \
    "s_cbranch_vccz LokPW1_%=\n" \
    "s_add_u32 s84, s84, 0x100000\n" \
    "s_waitcnt vmcnt(0)\n" \
    "v_max3_u32 v248, v134, v135, v136\n" \
    "v_max_u32 v248, v248, v137\n" \
    "v_cmp_eq_u32 vcc, -1, v248\n" \
    "s_cbranch_vccz LokPW1_%=\n" \
    "s_add_u32 s84, s84, 0x100000\n" \
    "s_branch LslowPW1_%=\n" \
    "LokPW1_%=:\n" \
    "s_nop 0\n" \
    "v_cndmask_b32 v62, v136, v134, s[58:59]\n" \
    "v_cndmask_b32 v63, v137, v135, s[58:59]\n" \
    "v_cndmask_b32 v64, v134, v136, s[58:59]\n" \
    "v_cndmask_b32 v65, v135, v137, s[58:59]\n" \
    "v_mov_b32_dpp v62, v62 row_ror:8 row_mask:0xf bank_mask:0xf bound_ctrl:1\n" \
    "v_mov_b32_dpp v63, v63 row_ror:8 row_mask:0xf bank_mask:0xf bound_ctrl:1\n" \
    "v_lshlrev_b32 v54, 16, v64\n" \
    "v_and_b32 v55, 0xffff0000, v64\n" \
    "v_lshlrev_b32 v56, 16, v65\n" \
    "v_and_b32 v57, 0xffff0000, v65\n" \
    "v_lshlrev_b32 v58, 16, v62\n" \
    "v_and_b32 v59, 0xffff0000, v62\n" \
    "v_lshlrev_b32 v60, 16, v63\n" \
    "v_and_b32 v61, 0xffff0000, v63\n" \
    "v_pk_add_f32 v[54:55], v[54:55], v[58:59]\n" \
    "v_pk_add_f32 v[56:57], v[56:57], v[60:61]\n" \
    "v_max_f32 v66, v54, v55\n" \
    "v_max3_f32 v66, v66, v56, v57\n" \
    "s_nop 1\n" \
    "v_max_f32_dpp v66, v66, v66 row_ror:8 row_mask:0xf bank_mask:0xf\n" \
    "v_pk_add_f32 v[62:63], v[54:55], v[66:67] op_sel_hi:[1,0] neg_lo:[0,1] neg_hi:[0,1]\n" \
    "v_pk_add_f32 v[64:65], v[56:57], v[66:67] op_sel_hi:[1,0] neg_lo:[0,1] neg_hi:[0,1]\n" \
    "v_exp_f32 v62, v62\n" \
    "v_exp_f32 v63, v63\n" \
    "v_exp_f32 v64, v64\n" \
    "v_exp_f32 v65, v65\n" \
    "s_nop 0\n" \
    "v_pk_add_f32 v[248:249], v[62:63], v[64:65]\n" \
    "v_add_f32 v68, v248, v249\n" \
    "s_nop 1\n" \
    "v_add_f32_dpp v68, v68, v68 row_ror:8 row_mask:0xf bank_mask:0xf\n" \
    "s_waitcnt vmcnt(1)\n" \
    "v_max3_u32 v248, v102, v103, v104\n" \
    "v_max3_u32 v249, v105, v110, v111\n" \
    "v_max3_u32 v248, v248, v112, v113\n" \
    "v_max3_u32 v249, v249, v142, v143\n" \
    "v_max_u32 v248, v248, v249\n" \
    "v_cmp_eq_u32 vcc, -1, v248\n" \
    "s_cbranch_vccz LokEW1_%=\n" \
    "s_add_u32 s84, s84, 0x400\n" \
    "s_branch LslowEW1_%=\n" \
    "LokEW1_%=:\n" \
    "s_nop 0\n" \
    "v_mov_b32_dpp v106, v102 row_ror:8 row_mask:0xf bank_mask:0xf bound_ctrl:1\n" \
    "v_mov_b32_dpp v107, v103 row_ror:8 row_mask:0xf bank_mask:0xf bound_ctrl:1\n" \
    "v_mov_b32_dpp v108, v104 row_ror:8 row_mask:0xf bank_mask:0xf bound_ctrl:1\n" \
    "v_mov_b32_dpp v109, v105 row_ror:8 row_mask:0xf bank_mask:0xf bound_ctrl:1\n" \
    "v_mfma_f32_16x16x32_bf16 v[118:121], v[86:89], v[102:105], 0\n" \
    "v_max_f32 v58, v66, v142\n" \
    "v_mfma_f32_16x16x32_bf16 v[126:129], v[94:97], v[110:113], 0\n" \
    "v_sub_f32 v59, v66, v58\n" \
    "v_sub_f32 v60, v142, v58\n" \
    "v_mfma_f32_16x16x32_bf16 v[122:125], v[90:93], v[106:109], 0\n" \
    "v_exp_f32 v59, v59\n" \
    "v_exp_f32 v60, v60\n" \
    "v_mov_b32_dpp v114, v110 row_ror:8 row_mask:0xf bank_mask:0xf bound_ctrl:1\n" \
    "v_mov_b32_dpp v115, v111 row_ror:8 row_mask:0xf bank_mask:0xf bound_ctrl:1\n" \
    "v_mov_b32_dpp v116, v112 row_ror:8 row_mask:0xf bank_mask:0xf bound_ctrl:1\n" \
    "v_mov_b32_dpp v117, v113 row_ror:8 row_mask:0xf bank_mask:0xf bound_ctrl:1\n" \
    "v_mul_f32 v61, v59, v68\n" \
    "s_nop 0\n" \
    "v_mfma_f32_16x16x32_bf16 v[130:133], v[98:101], v[114:117], 0\n" \
    "v_fmac_f32 v61, v60, v143\n" \
    "v_rcp_f32 v61, v61\n" \
    "s_nop 0\n" \
    "v_mul_f32 v66, v59, v61\n" \
    "v_mul_f32 v250, v60, v61\n" \
    "v_pk_mul_f32 v[62:63], v[62:63], v[66:67] op_sel_hi:[1,0]\n" \
    "v_pk_mul_f32 v[64:65], v[64:65], v[66:67] op_sel_hi:[1,0]\n" \
    "v_cvt_pk_bf16_f32 v148, v62, v63\n" \
    "v_cvt_pk_bf16_f32 v149, v64, v65\n" \
    "v_mov_b32 v54, v250\n" \
    "v_mov_b32_dpp v150, v148 row_ror:8 row_mask:0xf bank_mask:0xf bound_ctrl:1\n" \
    "v_mov_b32_dpp v151, v149 row_ror:8 row_mask:0xf bank_mask:0xf bound_ctrl:1\n" \
    "v_permlane16_swap_b32 v250, v54\n" \
    "s_nop 0\n" \
    "v_mfma_f32_16x16x32_bf16 v[62:65], v[144:147], v[148:151], 0\n" \
    "v_mov_b32 v58, v250\n" \
    "v_mov_b32 v56, v54\n" \
    "s_nop 0\n" \
    "v_permlane32_swap_b32 v250, v58\n" \
    "v_permlane32_swap_b32 v54, v56\n" \
    "v_pk_mul_f32 v[138:139], v[118:119], v[250:251] op_sel_hi:[1,0]\n" \
    "v_pk_mul_f32 v[140:141], v[120:121], v[250:251] op_sel_hi:[1,0]\n" \
    "v_pk_fma_f32 v[138:139], v[126:127], v[58:59], v[138:139] op_sel_hi:[1,0,1]\n" \
    "v_pk_fma_f32 v[140:141], v[128:129], v[58:59], v[140:141] op_sel_hi:[1,0,1]\n" \
    "v_pk_fma_f32 v[138:139], v[122:123], v[54:55], v[138:139] op_sel_hi:[1,0,1]\n" \
    "v_pk_fma_f32 v[140:141], v[124:125], v[54:55], v[140:141] op_sel_hi:[1,0,1]\n" \
    "v_pk_fma_f32 v[138:139], v[130:131], v[56:57], v[138:139] op_sel_hi:[1,0,1]\n" \
    "v_pk_fma_f32 v[140:141], v[132:133], v[56:57], v[140:141] op_sel_hi:[1,0,1]\n" \
    "v_pk_add_f32 v[138:139], v[138:139], v[62:63]\n" \
    "v_pk_add_f32 v[140:141], v[140:141], v[64:65]\n" \
    "s_mov_b64 exec, s[74:75]\n" \
    "ds_write_b128 v14, v[138:141]\n" \
    "s_mov_b64 exec, -1\n" \
    "LskipW1_%=:\n" \
    "s_waitcnt lgkmcnt(0)\n" \
    "s_barrier\n" \
    "s_add_u32 s60, s60, 1\n" \
    "s_mov_b32 s61, s62\n" \
    "s_add_u32 s67, s60, 1\n" \
    "s_lshr_b32 s68, s67, 5\n" \
    "s_lshl_b32 s68, s68, 4\n" \
    "s_lshr_b32 s68, s72, s68\n" \
    "s_and_b32 s68, s68, 0x2000\n" \
    "s_lshl_b32 s67, s67, 16\n" \
    "s_add_u32 s62, s67, s68\n" \
    "s_add_u32 s63, s63, 458752\n" \
    "s_add_u32 s64, s64, 131072\n" \
    "s_add_u32 s65, s65, 114688\n" \
    "s_cmp_ge_u32 s60, s70\n" \
    "s_cbranch_scc1 LendW1_%=\n" \
    "v_readfirstlane_b32 s67, v254\n" \
    "s_nop 1\n" \
    "s_cmp_lt_u32 s67, 160\n" \
    "s_cbranch_scc1 LslowTW1_%=\n" \
    "LbackTW1_%=:\n" \
    "s_mov_b32 s67, s66\n" \
    "s_cmp_eq_u32 s67, 0\n" \
    "s_cbranch_scc1 LdAW1_%=_e\n" \
    "LdAW1_%=:\n" \
    "s_sub_u32 s67, s67, 1\n" \
    "s_cmp_lg_u32 s67, 0\n" \
    "s_cbranch_scc1 LdAW1_%=\n" \
    "LdAW1_%=_e:\n" \
    "s_branch LloopW1_%=\n" \
    "LslowMW1_%=:\n" \
    "s_mov_b32 s87, 0\n" \
    "buffer_load_dwordx4 v[26:29], v3, s[36:39], s61 offen sc1\n" \
    "buffer_load_dwordx4 v[30:33], v4, s[36:39], s61 offen sc1\n" \
    "s_nop 15\n" \
    "s_nop 15\n" \
    "s_nop 7\n" \
    "LslowMlW1_%=:\n" \
    "s_add_u32 s84, s84, 1\n" \
    "buffer_load_dwordx4 v[26:29], v3, s[36:39], s61 offen sc1\n" \
    "buffer_load_dwordx4 v[30:33], v4, s[36:39], s61 offen sc1\n" \
    "s_waitcnt vmcnt(2)\n" \
    "v_max3_u32 v248, v26, v27, v28\n" \
    "v_max3_u32 v249, v29, v30, v31\n" \
    "v_max3_u32 v248, v248, v32, v33\n" \
    "v_max_u32 v248, v248, v249\n" \
    "v_cmp_eq_u32 vcc, -1, v248\n" \
    "s_cbranch_vccz LokMW1_%=\n" \
    "s_add_u32 s87, s87, 1\n" \
    "s_cmp_lt_u32 s87, 0x8000\n" \
    "s_cbranch_scc1 LslowMlW1_%=\n" \
    "s_branch LokMW1_%=\n" \
    "LslowEW1_%=:\n" \
    "s_mov_b32 s87, 0\n" \
    "buffer_load_dwordx4 v[102:105], v11, s[36:39], s61 offen sc1\n" \
    "buffer_load_dwordx4 v[110:113], v11, s[36:39], s61 offen offset:1024 sc1\n" \
    "buffer_load_dwordx2 v[142:143], v12, s[36:39], s61 offen sc1\n" \
    "s_nop 15\n" \
    "s_nop 15\n" \
    "s_nop 7\n" \
    "LslowElW1_%=:\n" \
    "s_add_u32 s84, s84, 1024\n" \
    "buffer_load_dwordx4 v[102:105], v11, s[36:39], s61 offen sc1\n" \
    "buffer_load_dwordx4 v[110:113], v11, s[36:39], s61 offen offset:1024 sc1\n" \
    "buffer_load_dwordx2 v[142:143], v12, s[36:39], s61 offen sc1\n" \
    "s_waitcnt vmcnt(3)\n" \
    "v_max3_u32 v248, v102, v103, v104\n" \
    "v_max3_u32 v249, v105, v110, v111\n" \
    "v_max3_u32 v248, v248, v112, v113\n" \
    "v_max3_u32 v249, v249, v142, v143\n" \
    "v_max_u32 v248, v248, v249\n" \
    "v_cmp_eq_u32 vcc, -1, v248\n" \
    "s_cbranch_vccz LokEW1_%=\n" \
    "s_add_u32 s87, s87, 1\n" \
    "s_cmp_lt_u32 s87, 0x8000\n" \
    "s_cbranch_scc1 LslowElW1_%=\n" \
    "s_branch LokEW1_%=\n" \
    "LslowPW1_%=:\n" \
    "s_mov_b32 s87, 0\n" \
    "buffer_load_dwordx4 v[134:137], v13, s[36:39], s61 offen sc1\n" \
    "s_nop 15\n" \
    "s_nop 15\n" \
    "s_nop 7\n" \
    "LslowPlW1_%=:\n" \
    "s_add_u32 s84, s84, 1048576\n" \
    "buffer_load_dwordx4 v[134:137], v13, s[36:39], s61 offen sc1\n" \
    "s_waitcnt vmcnt(1)\n" \
    "v_max3_u32 v248, v134, v135, v136\n" \
    "v_max_u32 v248, v248, v137\n" \
    "v_cmp_eq_u32 vcc, -1, v248\n" \
    "s_cbranch_vccz LokPW1_%=\n" \
    "s_add_u32 s87, s87, 1\n" \
    "s_cmp_lt_u32 s87, 0x8000\n" \
    "s_cbranch_scc1 LslowPlW1_%=\n" \
    "s_branch LokPW1_%=\n" \
    "LslowTW1_%=:\n" \
    "s_mov_b32 s87, 0\n" \
    "s_lshr_b32 s68, s60, 2\n" \
    "s_lshl_b32 s68, s68, 8\n" \
    "v_mov_b32 v253, s68\n" \
    "LslowTlW1_%=:\n" \
    "global_load_dword v254, v253, s[40:41] sc1\n" \
    "s_waitcnt vmcnt(0)\n" \
    "v_readfirstlane_b32 s67, v254\n" \
    "s_nop 1\n" \
    "s_cmp_ge_u32 s67, 160\n" \
    "s_cbranch_scc1 LbackTW1_%=\n" \
    "s_sleep 2\n" \
    "s_add_u32 s87, s87, 1\n" \
    "s_cmp_lt_u32 s87, 0x40000\n" \
    "s_cbranch_scc1 LslowTlW1_%=\n" \
    "s_branch LbackTW1_%=\n" \
    "LendW1_%=:\n" \
    "s_waitcnt vmcnt(0)\n" \
    "s_barrier\n" \
    "s_branch Ldone_%=\n" \
    "LwW2_%=:\n" \
    "v_add_u32 v248, 57344, v248\n" \
    "ds_read_b32 v0, v248\n" \
    "ds_read_b32 v1, v248 offset:256\n" \
    "ds_read_b32 v2, v248 offset:512\n" \
    "ds_read_b32 v3, v248 offset:768\n" \
    "ds_read_b32 v4, v248 offset:1024\n" \
    "ds_read_b32 v5, v248 offset:1280\n" \
    "ds_read_b32 v6, v248 offset:1536\n" \
    "ds_read_b32 v7, v248 offset:1792\n" \
    "s_waitcnt lgkmcnt(0)\n" \
    "ds_read_b32 v8, v248 offset:2048\n" \
    "ds_read_b32 v9, v248 offset:2304\n" \
    "ds_read_b32 v10, v248 offset:2560\n" \
    "ds_read_b32 v11, v248 offset:2816\n" \
    "ds_read_b32 v12, v248 offset:3072\n" \
    "ds_read_b32 v13, v248 offset:3328\n" \
    "ds_read_b32 v14, v248 offset:3584\n" \
    "ds_read_b32 v15, v248 offset:3840\n" \
    "s_waitcnt lgkmcnt(0)\n" \
    "ds_read_b32 v16, v248 offset:4096\n" \
    "ds_read_b32 v17, v248 offset:4352\n" \
    "ds_read_b32 v18, v248 offset:4608\n" \
    "ds_read_b32 v19, v248 offset:4864\n" \
    "ds_read_b32 v20, v248 offset:5120\n" \
    "ds_read_b32 v21, v248 offset:5376\n" \
    "ds_read_b32 v22, v248 offset:5632\n" \
    "ds_read_b32 v23, v248 offset:5888\n" \
    "s_waitcnt lgkmcnt(0)\n" \
    "ds_read_b32 v24, v248 offset:6144\n" \
    "s_waitcnt lgkmcnt(0)\n" \
    "s_add_u32 s67, s85, 117760\n" \
    "buffer_load_dwordx4 a[0:3], v20, s[88:91], s67 offen\n" \
    "s_add_u32 s67, s85, 146432\n" \
    "buffer_load_dwordx4 a[4:7], v20, s[88:91], s67 offen\n" \
    "s_add_u32 s67, s85, 347136\n" \
    "buffer_load_dwordx4 a[8:11], v20, s[88:91], s67 offen\n" \
    "s_add_u32 s67, s85, 375808\n" \
    "buffer_load_dwordx4 a[12:15], v20, s[88:91], s67 offen\n" \
    "s_add_u32 s67, s85, 3072\n" \
    "buffer_load_dwordx4 a[16:19], v20, s[88:91], s67 offen\n" \
    "s_add_u32 s67, s85, 31744\n" \
    "buffer_load_dwordx4 a[20:23], v20, s[88:91], s67 offen\n" \
    "s_add_u32 s67, s85, 60416\n" \
    "buffer_load_dwordx4 a[24:27], v20, s[88:91], s67 offen\n" \
    "s_add_u32 s67, s85, 89088\n" \
    "buffer_load_dwordx4 a[28:31], v20, s[88:91], s67 offen\n" \
    "s_add_u32 s67, s85, 175104\n" \
    "buffer_load_dwordx4 a[32:35], v20, s[88:91], s67 offen\n" \
    "s_add_u32 s67, s85, 203776\n" \
    "buffer_load_dwordx4 a[36:39], v20, s[88:91], s67 offen\n" \
    "s_add_u32 s67, s85, 232448\n" \
    "buffer_load_dwordx4 a[40:43], v20, s[88:91], s67 offen\n" \
    "s_add_u32 s67, s85, 261120\n" \
    "buffer_load_dwordx4 a[44:47], v20, s[88:91], s67 offen\n" \
    "s_add_u32 s67, s85, 289792\n" \
    "buffer_load_dwordx4 a[48:51], v20, s[88:91], s67 offen\n" \
    "s_add_u32 s67, s85, 318464\n" \
    "buffer_load_dwordx4 a[52:55], v20, s[88:91], s67 offen\n" \
    "s_add_u32 s67, s85, 117760\n" \
    "buffer_load_dwordx4 a[56:59], v21, s[88:91], s67 offen\n" \
    "s_add_u32 s67, s85, 146432\n" \
    "buffer_load_dwordx4 a[60:63], v21, s[88:91], s67 offen\n" \
    "s_add_u32 s67, s85, 347136\n" \
    "buffer_load_dwordx4 a[64:67], v21, s[88:91], s67 offen\n" \
    "s_add_u32 s67, s85, 375808\n" \
    "buffer_load_dwordx4 a[68:71], v21, s[88:91], s67 offen\n" \
    "s_add_u32 s67, s85, 3072\n" \
    "buffer_load_dwordx4 a[72:75], v21, s[88:91], s67 offen\n" \
    "s_add_u32 s67, s85, 31744\n" \
    "buffer_load_dwordx4 a[76:79], v21, s[88:91], s67 offen\n" \
    "s_add_u32 s67, s85, 60416\n" \
    "buffer_load_dwordx4 a[80:83], v21, s[88:91], s67 offen\n" \
    "s_add_u32 s67, s85, 89088\n" \
    "buffer_load_dwordx4 a[84:87], v21, s[88:91], s67 offen\n" \
    "s_add_u32 s67, s85, 175104\n" \
    "buffer_load_dwordx4 a[88:91], v21, s[88:91], s67 offen\n" \
    "s_add_u32 s67, s85, 203776\n" \
    "buffer_load_dwordx4 a[92:95], v21, s[88:91], s67 offen\n" \
    "s_add_u32 s67, s85, 232448\n" \
    "buffer_load_dwordx4 a[96:99], v21, s[88:91], s67 offen\n" \
    "s_add_u32 s67, s85, 261120\n" \
    "buffer_load_dwordx4 a[100:103], v21, s[88:91], s67 offen\n" \
    "s_add_u32 s67, s85, 289792\n" \
    "buffer_load_dwordx4 a[104:107], v21, s[88:91], s67 offen\n" \
    "s_add_u32 s67, s85, 318464\n" \
    "buffer_load_dwordx4 a[108:111], v21, s[88:91], s67 offen\n" \
    "s_add_u32 s67, s85, 118784\n" \
    "buffer_load_dwordx4 a[112:115], v20, s[88:91], s67 offen\n" \
    "s_add_u32 s67, s85, 147456\n" \
    "buffer_load_dwordx4 a[116:119], v20, s[88:91], s67 offen\n" \
    "s_add_u32 s67, s85, 348160\n" \
    "buffer_load_dwordx4 a[120:123], v20, s[88:91], s67 offen\n" \
    "s_add_u32 s67, s85, 376832\n" \
    "buffer_load_dwordx4 a[124:127], v20, s[88:91], s67 offen\n" \
    "s_add_u32 s67, s85, 4096\n" \
    "buffer_load_dwordx4 a[128:131], v20, s[88:91], s67 offen\n" \
    "s_add_u32 s67, s85, 32768\n" \
    "buffer_load_dwordx4 a[132:135], v20, s[88:91], s67 offen\n" \
    "s_add_u32 s67, s85, 61440\n" \
    "buffer_load_dwordx4 a[136:139], v20, s[88:91], s67 offen\n" \
    "s_add_u32 s67, s85, 90112\n" \
    "buffer_load_dwordx4 a[140:143], v20, s[88:91], s67 offen\n" \
    "s_add_u32 s67, s85, 176128\n" \
    "buffer_load_dwordx4 a[144:147], v20, s[88:91], s67 offen\n" \
    "s_add_u32 s67, s85, 204800\n" \
    "buffer_load_dwordx4 a[148:151], v20, s[88:91], s67 offen\n" \
    "s_add_u32 s67, s85, 233472\n" \
    "buffer_load_dwordx4 a[152:155], v20, s[88:91], s67 offen\n" \
    "s_add_u32 s67, s85, 262144\n" \
    "buffer_load_dwordx4 a[156:159], v20, s[88:91], s67 offen\n" \
    "s_add_u32 s67, s85, 290816\n" \
    "buffer_load_dwordx4 a[160:163], v20, s[88:91], s67 offen\n" \
    "s_add_u32 s67, s85, 319488\n" \
    "buffer_load_dwordx4 a[164:167], v20, s[88:91], s67 offen\n" \
    "s_add_u32 s67, s85, 350208\n" \
    "buffer_load_dwordx4 a[168:171], v20, s[88:91], s67 offen\n" \
    "s_add_u32 s67, s85, 378880\n" \
    "buffer_load_dwordx4 a[172:175], v20, s[88:91], s67 offen\n" \
    "s_add_u32 s67, s85, 235520\n" \
    "buffer_load_dwordx4 a[176:179], v20, s[88:91], s67 offen\n" \
    "s_add_u32 s67, s85, 264192\n" \
    "buffer_load_dwordx4 a[180:183], v20, s[88:91], s67 offen\n" \
    "s_add_u32 s67, s85, 292864\n" \
    "buffer_load_dwordx4 a[184:187], v20, s[88:91], s67 offen\n" \
    "s_add_u32 s67, s85, 321536\n" \
    "buffer_load_dwordx4 a[188:191], v20, s[88:91], s67 offen\n" \
    "s_waitcnt vmcnt(0)\n" \
    "s_add_u32 s67, s86, 3325952\n" \
    "buffer_load_dwordx4 a[200:203], v22, s[92:95], s67 offen\n" \
    "s_add_u32 s67, s86, 3354624\n" \
    "buffer_load_dwordx4 a[204:207], v22, s[92:95], s67 offen\n" \
    "s_add_u32 s67, s86, 3555328\n" \
    "buffer_load_dwordx4 a[208:211], v22, s[92:95], s67 offen\n" \
    "s_add_u32 s67, s86, 3584000\n" \
    "buffer_load_dwordx4 a[212:215], v22, s[92:95], s67 offen\n" \
    "s_add_u32 s67, s86, 3211264\n" \
    "buffer_load_dwordx4 a[216:219], v22, s[92:95], s67 offen\n" \
    "s_add_u32 s67, s86, 3239936\n" \
    "buffer_load_dwordx4 a[220:223], v22, s[92:95], s67 offen\n" \
    "s_add_u32 s67, s86, 3268608\n" \
    "buffer_load_dwordx4 a[224:227], v22, s[92:95], s67 offen\n" \
    "s_add_u32 s67, s86, 3297280\n" \
    "buffer_load_dwordx4 a[228:231], v22, s[92:95], s67 offen\n" \
    "s_add_u32 s67, s86, 3383296\n" \
    "buffer_load_dwordx4 a[232:235], v22, s[92:95], s67 offen\n" \
    "s_add_u32 s67, s86, 3411968\n" \
    "buffer_load_dwordx4 a[236:239], v22, s[92:95], s67 offen\n" \
    "s_add_u32 s67, s86, 3440640\n" \
    "buffer_load_dwordx4 a[240:243], v22, s[92:95], s67 offen\n" \
    "s_add_u32 s67, s86, 3469312\n" \
    "buffer_load_dwordx4 a[244:247], v22, s[92:95], s67 offen\n" \
    "s_add_u32 s67, s86, 3497984\n" \
    "buffer_load_dwordx4 a[248:251], v22, s[92:95], s67 offen\n" \
    "s_add_u32 s67, s86, 3526656\n" \
    "buffer_load_dwordx4 a[252:255], v22, s[92:95], s67 offen\n" \
    "s_add_u32 s67, s86, 3727360\n" \
    "buffer_load_dwordx4 v[192:195], v22, s[92:95], s67 offen\n" \
    "s_add_u32 s67, s86, 3756032\n" \
    "buffer_load_dwordx4 v[196:199], v22, s[92:95], s67 offen\n" \
    "s_add_u32 s67, s86, 3956736\n" \
    "buffer_load_dwordx4 v[200:203], v22, s[92:95], s67 offen\n" \
    "s_add_u32 s67, s86, 3985408\n" \
    "buffer_load_dwordx4 v[204:207], v22, s[92:95], s67 offen\n" \
    "s_add_u32 s67, s86, 3612672\n" \
    "buffer_load_dwordx4 v[208:211], v22, s[92:95], s67 offen\n" \
    "s_add_u32 s67, s86, 3641344\n" \
    "buffer_load_dwordx4 v[212:215], v22, s[92:95], s67 offen\n" \
    "s_add_u32 s67, s86, 3670016\n" \
    "buffer_load_dwordx4 v[216:219], v22, s[92:95], s67 offen\n" \
    "s_add_u32 s67, s86, 3698688\n" \
    "buffer_load_dwordx4 v[220:223], v22, s[92:95], s67 offen\n" \
    "s_add_u32 s67, s86, 3784704\n" \
    "buffer_load_dwordx4 v[224:227], v22, s[92:95], s67 offen\n" \
    "s_add_u32 s67, s86, 3813376\n" \
    "buffer_load_dwordx4 v[228:231], v22, s[92:95], s67 offen\n" \
    "s_add_u32 s67, s86, 3842048\n" \
    "buffer_load_dwordx4 v[232:235], v22, s[92:95], s67 offen\n" \
    "s_add_u32 s67, s86, 3870720\n" \
    "buffer_load_dwordx4 v[236:239], v22, s[92:95], s67 offen\n" \
    "s_add_u32 s67, s86, 3899392\n" \
    "buffer_load_dwordx4 v[240:243], v22, s[92:95], s67 offen\n" \
    "s_add_u32 s67, s86, 3928064\n" \
    "buffer_load_dwordx4 v[244:247], v22, s[92:95], s67 offen\n" \
    "s_add_u32 s67, s86, 4128768\n" \
    "buffer_load_dwordx4 v[152:155], v22, s[92:95], s67 offen\n" \
    "s_add_u32 s67, s86, 4157440\n" \
    "buffer_load_dwordx4 v[156:159], v22, s[92:95], s67 offen\n" \
    "s_add_u32 s67, s86, 4358144\n" \
    "buffer_load_dwordx4 v[160:163], v22, s[92:95], s67 offen\n" \
    "s_add_u32 s67, s86, 4386816\n" \
    "buffer_load_dwordx4 v[164:167], v22, s[92:95], s67 offen\n" \
    "s_add_u32 s67, s86, 4014080\n" \
    "buffer_load_dwordx4 v[168:171], v22, s[92:95], s67 offen\n" \
    "s_add_u32 s67, s86, 4042752\n" \
    "buffer_load_dwordx4 v[172:175], v22, s[92:95], s67 offen\n" \
    "s_add_u32 s67, s86, 4071424\n" \
    "buffer_load_dwordx4 v[176:179], v22, s[92:95], s67 offen\n" \
    "s_add_u32 s67, s86, 4100096\n" \
    "buffer_load_dwordx4 v[180:183], v22, s[92:95], s67 offen\n" \
    "s_add_u32 s67, s86, 4186112\n" \
    "buffer_load_dwordx4 v[184:187], v22, s[92:95], s67 offen\n" \
    "s_add_u32 s67, s86, 4214784\n" \
    "buffer_load_dwordx4 v[188:191], v22, s[92:95], s67 offen\n" \
    "s_waitcnt vmcnt(0)\n" \
    "ds_write_b128 v17, v[152:155]\n" \
    "ds_write_b128 v17, v[156:159] offset:1024\n" \
    "ds_write_b128 v17, v[160:163] offset:2048\n" \
    "ds_write_b128 v17, v[164:167] offset:3072\n" \
    "ds_write_b128 v17, v[168:171] offset:4096\n" \
    "ds_write_b128 v17, v[172:175] offset:5120\n" \
    "ds_write_b128 v17, v[176:179] offset:6144\n" \
    "ds_write_b128 v17, v[180:183] offset:7168\n" \
    "ds_write_b128 v17, v[184:187] offset:8192\n" \
    "ds_write_b128 v17, v[188:191] offset:9216\n" \
    "s_waitcnt lgkmcnt(0)\n" \
    "s_add_u32 s67, s86, 4243456\n" \
    "buffer_load_dwordx4 v[152:155], v22, s[92:95], s67 offen\n" \
    "s_add_u32 s67, s86, 4272128\n" \
    "buffer_load_dwordx4 v[156:159], v22, s[92:95], s67 offen\n" \
    "s_add_u32 s67, s86, 4300800\n" \
    "buffer_load_dwordx4 v[160:163], v22, s[92:95], s67 offen\n" \
    "s_add_u32 s67, s86, 4329472\n" \
    "buffer_load_dwordx4 v[164:167], v22, s[92:95], s67 offen\n" \
    "s_add_u32 s67, s86, 4530176\n" \
    "buffer_load_dwordx4 v[168:171], v22, s[92:95], s67 offen\n" \
    "s_add_u32 s67, s86, 4558848\n" \
    "buffer_load_dwordx4 v[172:175], v22, s[92:95], s67 offen\n" \
    "s_add_u32 s67, s86, 4759552\n" \
    "buffer_load_dwordx4 v[176:179], v22, s[92:95], s67 offen\n" \
    "s_add_u32 s67, s86, 4788224\n" \
    "buffer_load_dwordx4 v[180:183], v22, s[92:95], s67 offen\n" \
    "s_add_u32 s67, s86, 4415488\n" \
    "buffer_load_dwordx4 v[184:187], v22, s[92:95], s67 offen\n" \
    "s_add_u32 s67, s86, 4444160\n" \
    "buffer_load_dwordx4 v[188:191], v22, s[92:95], s67 offen\n" \
    "s_waitcnt vmcnt(0)\n" \
    "ds_write_b128 v17, v[152:155] offset:10240\n" \
    "ds_write_b128 v17, v[156:159] offset:11264\n" \
    "ds_write_b128 v17, v[160:163] offset:12288\n" \
    "ds_write_b128 v17, v[164:167] offset:13312\n" \
    "ds_write_b128 v17, v[168:171] offset:14336\n" \
    "ds_write_b128 v17, v[172:175] offset:15360\n" \
    "ds_write_b128 v17, v[176:179] offset:16384\n" \
    "ds_write_b128 v17, v[180:183] offset:17408\n" \
    "ds_write_b128 v17, v[184:187] offset:18432\n" \
    "ds_write_b128 v17, v[188:191] offset:19456\n" \
    "s_waitcnt lgkmcnt(0)\n" \
    "s_add_u32 s67, s86, 4472832\n" \
    "buffer_load_dwordx4 v[152:155], v22, s[92:95], s67 offen\n" \
    "s_add_u32 s67, s86, 4501504\n" \
    "buffer_load_dwordx4 v[156:159], v22, s[92:95], s67 offen\n" \
    "s_add_u32 s67, s86, 4587520\n" \
    "buffer_load_dwordx4 v[160:163], v22, s[92:95], s67 offen\n" \
    "s_add_u32 s67, s86, 4616192\n" \
    "buffer_load_dwordx4 v[164:167], v22, s[92:95], s67 offen\n" \
    "s_add_u32 s67, s86, 4644864\n" \
    "buffer_load_dwordx4 v[168:171], v22, s[92:95], s67 offen\n" \
    "s_add_u32 s67, s86, 4673536\n" \
    "buffer_load_dwordx4 v[172:175], v22, s[92:95], s67 offen\n" \
    "s_add_u32 s67, s86, 4702208\n" \
    "buffer_load_dwordx4 v[176:179], v22, s[92:95], s67 offen\n" \
    "s_add_u32 s67, s86, 4730880\n" \
    "buffer_load_dwordx4 v[180:183], v22, s[92:95], s67 offen\n" \
    "s_waitcnt vmcnt(0)\n" \
    "ds_write_b128 v17, v[152:155] offset:20480\n" \
    "ds_write_b128 v17, v[156:159] offset:21504\n" \
    "ds_write_b128 v17, v[160:163] offset:22528\n" \
    "ds_write_b128 v17, v[164:167] offset:23552\n" \
    "ds_write_b128 v17, v[168:171] offset:24576\n" \
    "ds_write_b128 v17, v[172:175] offset:25600\n" \
    "ds_write_b128 v17, v[176:179] offset:26624\n" \
    "ds_write_b128 v17, v[180:183] offset:27648\n" \
    "s_waitcnt lgkmcnt(0)\n" \
    "s_mov_b32 s87, 0\n" \
    "Lt0W2_%=:\n" \
    "v_mov_b32 v248, 0\n" \
    "global_load_dword v254, v248, s[40:41] sc1\n" \
    "s_waitcnt vmcnt(0)\n" \
    "v_readfirstlane_b32 s67, v254\n" \
    "s_nop 1\n" \
    "s_cmp_ge_u32 s67, 160\n" \
    "s_cbranch_scc1 Lt0dW2_%=\n" \
    "s_sleep 4\n" \
    "s_add_u32 s87, s87, 1\n" \
    "s_cmp_lt_u32 s87, 0x40000\n" \
    "s_cbranch_scc1 Lt0W2_%=\n" \
    "Lt0dW2_%=:\n" \
    "s_mov_b32 s87, 0\n" \
    "Lp0W2_%=:\n" \
    "v_mov_b32 v248, 45120\n" \
    "global_load_dword v254, v248, s[40:41] sc1\n" \
    "s_waitcnt vmcnt(0)\n" \
    "v_readfirstlane_b32 s72, v254\n" \
    "s_nop 1\n" \
    "s_cmp_lg_u32 s72, -1\n" \
    "s_cbranch_scc1 Lp0dW2_%=\n" \
    "s_add_u32 s87, s87, 1\n" \
    "s_cmp_lt_u32 s87, 0x100000\n" \
    "s_cbranch_scc1 Lp0W2_%=\n" \
    "s_mov_b32 s72, 0\n" \
    "Lp0dW2_%=:\n" \
    "s_and_b32 s61, s72, 0x2000\n" \
    "s_add_u32 s62, s61, 65536\n" \
    "s_waitcnt vmcnt(0)\n" \
    "s_waitcnt lgkmcnt(0)\n" \
    "s_barrier\n" \
    "s_memrealtime s[96:97]\n" \
    "s_waitcnt lgkmcnt(0)\n" \
    "LloopW2_%=:\n" \
    "buffer_load_dwordx4 v[26:29], v3, s[36:39], s61 offen sc1\n" \
    "buffer_load_dwordx4 v[30:33], v4, s[36:39], s61 offen sc1\n" \
    "v_mov_b32 v54, 0\n" \
    "v_mov_b32 v55, 0\n" \
    "v_mov_b32 v56, 0\n" \
    "v_mov_b32 v57, 0\n" \
    "v_mov_b32 v58, 0\n" \
    "v_mov_b32 v59, 0\n" \
    "v_mov_b32 v60, 0\n" \
    "v_mov_b32 v61, 0\n" \
    "v_mov_b32 v62, 0\n" \
    "v_mov_b32 v63, 0\n" \
    "v_mov_b32 v64, 0\n" \
    "v_mov_b32 v65, 0\n" \
    "v_mov_b32 v66, 0\n" \
    "v_mov_b32 v67, 0\n" \
    "v_mov_b32 v68, 0\n" \
    "v_mov_b32 v69, 0\n" \
    "v_mov_b32 v70, 0\n" \
    "v_mov_b32 v71, 0\n" \
    "v_mov_b32 v72, 0\n" \
    "v_mov_b32 v73, 0\n" \
    "v_mov_b32 v74, 0\n" \
    "v_mov_b32 v75, 0\n" \
    "v_mov_b32 v76, 0\n" \
    "v_mov_b32 v77, 0\n" \
    "v_mov_b32 v78, 0\n" \
    "v_mov_b32 v79, 0\n" \
    "v_mov_b32 v80, 0\n" \
    "v_mov_b32 v81, 0\n" \
    "v_mov_b32 v82, 0\n" \
    "v_mov_b32 v83, 0\n" \
    "v_mov_b32 v84, 0\n" \
    "v_mov_b32 v85, 0\n" \
    "s_nop 15\n" \
    "s_nop 7\n" \
    "buffer_load_dwordx4 v[26:29], v3, s[36:39], s61 offen sc1\n" \
    "buffer_load_dwordx4 v[30:33], v4, s[36:39], s61 offen sc1\n" \
    "s_waitcnt vmcnt(2)\n" \
    "v_max3_u32 v248, v26, v27, v28\n" \
    "v_max3_u32 v249, v29, v30, v31\n" \
    "v_max3_u32 v248, v248, v32, v33\n" \
    "v_max_u32 v248, v248, v249\n" \
    "v_cmp_eq_u32 vcc, -1, v248\n" \
    "s_cbranch_vccz LokMW2_%=\n" \
    "s_add_u32 s84, s84, 1\n" \
    "s_waitcnt vmcnt(0)\n" \
    "v_max3_u32 v248, v26, v27, v28\n" \
    "v_max3_u32 v249, v29, v30, v31\n" \
    "v_max3_u32 v248, v248, v32, v33\n" \
    "v_max_u32 v248, v248, v249\n" \
    "v_cmp_eq_u32 vcc, -1, v248\n" \
    "s_cbranch_vccz LokMW2_%=\n" \
    "s_add_u32 s84, s84, 1\n" \
    "s_branch LslowMW2_%=\n" \
    "LokMW2_%=:\n" \
    "s_nop 0\n" \
    "ds_write_b128 v5, v[26:29] offset:2048\n" \
    "ds_write_b128 v5, v[30:33] offset:6144\n" \
    "v_smfmac_f32_16x16x64_bf16 v[54:57], v[26:29], a[0:7], v1\n" \
    "v_smfmac_f32_16x16x64_bf16 v[58:61], v[26:29], a[56:63], v1\n" \
    "v_smfmac_f32_16x16x64_bf16 v[62:65], v[26:29], a[112:119], v1\n" \
    "s_waitcnt lgkmcnt(0)\n" \
    "s_barrier\n" \
    "ds_read_b128 v[34:37], v5\n" \
    "ds_read_b128 v[38:41], v5 offset:1024\n" \
    "ds_read_b128 v[42:45], v5 offset:3072\n" \
    "ds_read_b128 v[46:49], v5 offset:4096\n" \
    "ds_read_b128 v[50:53], v5 offset:5120\n" \
    "v_smfmac_f32_16x16x64_bf16 v[54:57], v[30:33], a[8:15], v1\n" \
    "v_smfmac_f32_16x16x64_bf16 v[58:61], v[30:33], a[64:71], v1\n" \
    "v_smfmac_f32_16x16x64_bf16 v[62:65], v[30:33], a[120:127], v1\n" \
    "v_smfmac_f32_16x16x64_bf16 v[66:69], v[30:33], a[168:175], v1\n" \
    "s_waitcnt lgkmcnt(4)\n" \
    "v_smfmac_f32_16x16x64_bf16 v[54:57], v[34:37], a[16:23], v1\n" \
    "v_smfmac_f32_16x16x64_bf16 v[58:61], v[34:37], a[72:79], v1\n" \
    "v_smfmac_f32_16x16x64_bf16 v[62:65], v[34:37], a[128:135], v1\n" \
    "s_waitcnt lgkmcnt(1)\n" \
    "v_smfmac_f32_16x16x64_bf16 v[66:69], v[46:49], a[176:183], v1\n" \
    "s_waitcnt lgkmcnt(0)\n" \
    "v_smfmac_f32_16x16x64_bf16 v[66:69], v[50:53], a[184:191], v1\n" \
    "buffer_load_dwordx4 v[86:89], v0, s[44:47], s63 offen sc1\n" \
    "v_smfmac_f32_16x16x64_bf16 v[54:57], v[38:41], a[24:31], v1\n" \
    "v_smfmac_f32_16x16x64_bf16 v[54:57], v[42:45], a[32:39], v1\n" \
    "ds_read_b128 v[152:155], v17\n" \
    "ds_read_b128 v[156:159], v17 offset:1024\n" \
    "ds_read_b128 v[160:163], v17 offset:2048\n" \
    "v_smfmac_f32_16x16x64_bf16 v[54:57], v[46:49], a[40:47], v1\n" \
    "s_nop 0\n" \
    "v_add_f32 v250, v66, v67\n" \
    "v_add_f32 v251, v68, v69\n" \
    "v_cvt_pk_bf16_f32 v250, v250, v251\n" \
    "v_smfmac_f32_16x16x64_bf16 v[54:57], v[50:53], a[48:55], v1\n" \
    "buffer_load_dwordx4 v[90:93], v0, s[44:47], s63 offen offset:1024 sc1\n" \
    "ds_write_b32 v7, v250\n" \
    "ds_read_b128 v[164:167], v17 offset:3072\n" \
    "ds_read_b128 v[168:171], v17 offset:4096\n" \
    "v_smfmac_f32_16x16x64_bf16 v[58:61], v[38:41], a[80:87], v1\n" \
    "ds_read_b128 v[172:175], v17 offset:5120\n" \
    "ds_read_b128 v[176:179], v17 offset:6144\n" \
    "ds_read_b128 v[180:183], v17 offset:7168\n" \
    "v_smfmac_f32_16x16x64_bf16 v[58:61], v[42:45], a[88:95], v1\n" \
    "ds_read_b128 v[184:187], v17 offset:8192\n" \
    "ds_read_b128 v[188:191], v17 offset:9216\n" \
    "v_smfmac_f32_16x16x64_bf16 v[58:61], v[46:49], a[96:103], v1\n" \
    "v_add_f32 v248, v54, v55\n" \
    "v_add_f32 v249, v56, v57\n" \
    "v_cvt_pk_bf16_f32 v248, v248, v249\n" \
    "v_smfmac_f32_16x16x64_bf16 v[58:61], v[50:53], a[104:111], v1\n" \
    "buffer_load_dwordx4 v[94:97], v0, s[44:47], s63 offen offset:2048 sc1\n" \
    "ds_write_b32 v6, v248\n" \
    "v_smfmac_f32_16x16x64_bf16 v[62:65], v[38:41], a[136:143], v1\n" \
    "v_smfmac_f32_16x16x64_bf16 v[62:65], v[42:45], a[144:151], v1\n" \
    "v_smfmac_f32_16x16x64_bf16 v[62:65], v[46:49], a[152:159], v1\n" \
    "buffer_load_dwordx4 v[98:101], v0, s[44:47], s63 offen offset:3072 sc1\n" \
    "s_nop 1\n" \
    "v_add_f32 v250, v58, v59\n" \
    "v_add_f32 v251, v60, v61\n" \
    "v_cvt_pk_bf16_f32 v250, v250, v251\n" \
    "v_smfmac_f32_16x16x64_bf16 v[62:65], v[50:53], a[160:167], v1\n" \
    "ds_write_b32 v6, v250 offset:256\n" \
    "s_nop 6\n" \
    "v_add_f32 v248, v62, v63\n" \
    "v_add_f32 v249, v64, v65\n" \
    "v_cvt_pk_bf16_f32 v248, v248, v249\n" \
    "ds_write_b32 v6, v248 offset:512\n" \
    "s_waitcnt lgkmcnt(0)\n" \
    "s_barrier\n" \
    "s_memtime s[76:77]\n" \
    "s_waitcnt lgkmcnt(0)\n" \
    "ds_read_b64_tr_b16 v[118:119], v18\n" \
    "ds_read_b64_tr_b16 v[120:121], v18 offset:64\n" \
    "v_smfmac_f32_16x16x64_bf16 v[70:73], v[26:29], a[200:207], v1\n" \
    "v_smfmac_f32_16x16x64_bf16 v[78:81], v[26:29], v[152:159], v1\n" \
    "ds_read_b128 v[152:155], v17 offset:10240\n" \
    "ds_read_b128 v[156:159], v17 offset:11264\n" \
    "v_smfmac_f32_16x16x64_bf16 v[70:73], v[30:33], a[208:215], v1\n" \
    "v_smfmac_f32_16x16x64_bf16 v[78:81], v[30:33], v[160:167], v1\n" \
    "ds_read_b128 v[160:163], v17 offset:12288\n" \
    "ds_read_b128 v[164:167], v17 offset:13312\n" \
    "v_smfmac_f32_16x16x64_bf16 v[70:73], v[34:37], a[216:223], v1\n" \
    "v_smfmac_f32_16x16x64_bf16 v[78:81], v[34:37], v[168:175], v1\n" \
    "ds_read_b128 v[168:171], v17 offset:14336\n" \
    "ds_read_b128 v[172:175], v17 offset:15360\n" \
    "v_smfmac_f32_16x16x64_bf16 v[70:73], v[38:41], a[224:231], v1\n" \
    "v_smfmac_f32_16x16x64_bf16 v[78:81], v[38:41], v[176:183], v1\n" \
    "ds_read_b128 v[176:179], v17 offset:16384\n" \
    "ds_read_b128 v[180:183], v17 offset:17408\n" \
    "v_smfmac_f32_16x16x64_bf16 v[70:73], v[42:45], a[232:239], v1\n" \
    "v_smfmac_f32_16x16x64_bf16 v[78:81], v[42:45], v[184:191], v1\n" \
    "ds_read_b128 v[184:187], v17 offset:18432\n" \
    "ds_read_b128 v[188:191], v17 offset:19456\n" \
    "v_smfmac_f32_16x16x64_bf16 v[70:73], v[46:49], a[240:247], v1\n" \
    "s_waitcnt lgkmcnt(8)\n" \
    "v_smfmac_f32_16x16x64_bf16 v[78:81], v[46:49], v[152:159], v1\n" \
    "ds_read_b128 v[152:155], v17 offset:20480\n" \
    "ds_read_b128 v[156:159], v17 offset:21504\n" \
    "v_smfmac_f32_16x16x64_bf16 v[70:73], v[50:53], a[248:255], v1\n" \
    "s_add_u32 s67, s60, 1\n" \
    "s_lshr_b32 s67, s67, 2\n" \
    "s_min_u32 s67, s67, 15\n" \
    "s_lshl_b32 s67, s67, 8\n" \
    "v_mov_b32 v253, s67\n" \
    "global_load_dword v254, v253, s[40:41] sc1\n" \
    "s_waitcnt lgkmcnt(8)\n" \
    "v_smfmac_f32_16x16x64_bf16 v[78:81], v[50:53], v[160:167], v1\n" \
    "ds_read_b128 v[160:163], v17 offset:22528\n" \
    "ds_read_b128 v[164:167], v17 offset:23552\n" \
    "v_smfmac_f32_16x16x64_bf16 v[74:77], v[26:29], v[192:199], v1\n" \
    "s_waitcnt lgkmcnt(8)\n" \
    "v_smfmac_f32_16x16x64_bf16 v[82:85], v[26:29], v[168:175], v1\n" \
    "ds_read_b128 v[168:171], v17 offset:24576\n" \
    "ds_read_b128 v[172:175], v17 offset:25600\n" \
    "v_smfmac_f32_16x16x64_bf16 v[74:77], v[30:33], v[200:207], v1\n" \
    "s_waitcnt lgkmcnt(8)\n" \
    "v_smfmac_f32_16x16x64_bf16 v[82:85], v[30:33], v[176:183], v1\n" \
    "ds_read_b128 v[176:179], v17 offset:26624\n" \
    "ds_read_b128 v[180:183], v17 offset:27648\n" \
    "v_smfmac_f32_16x16x64_bf16 v[74:77], v[34:37], v[208:215], v1\n" \
    "s_waitcnt lgkmcnt(8)\n" \
    "v_smfmac_f32_16x16x64_bf16 v[82:85], v[34:37], v[184:191], v1\n" \
    "v_add_f32 v70, v70, v71\n" \
    "v_add_f32 v72, v72, v73\n" \
    "v_cvt_pk_bf16_f32 v144, v70, v72\n" \
    "v_smfmac_f32_16x16x64_bf16 v[74:77], v[38:41], v[216:223], v1\n" \
    "s_waitcnt lgkmcnt(6)\n" \
    "v_smfmac_f32_16x16x64_bf16 v[82:85], v[38:41], v[152:159], v1\n" \
    "v_add_f32 v78, v78, v79\n" \
    "v_add_f32 v80, v80, v81\n" \
    "v_cvt_pk_bf16_f32 v146, v78, v80\n" \
    "v_smfmac_f32_16x16x64_bf16 v[74:77], v[42:45], v[224:231], v1\n" \
    "s_waitcnt lgkmcnt(4)\n" \
    "v_smfmac_f32_16x16x64_bf16 v[82:85], v[42:45], v[160:167], v1\n" \
    "buffer_store_dwordx4 v[118:121], v19, s[48:51], s64 offen sc1\n" \
    "v_smfmac_f32_16x16x64_bf16 v[74:77], v[46:49], v[232:239], v1\n" \
    "s_waitcnt lgkmcnt(2)\n" \
    "v_smfmac_f32_16x16x64_bf16 v[82:85], v[46:49], v[168:175], v1\n" \
    "v_smfmac_f32_16x16x64_bf16 v[74:77], v[50:53], v[240:247], v1\n" \
    "s_waitcnt lgkmcnt(0)\n" \
    "v_smfmac_f32_16x16x64_bf16 v[82:85], v[50:53], v[176:183], v1\n" \
    "s_nop 5\n" \
    "v_add_f32 v74, v74, v75\n" \
    "v_add_f32 v76, v76, v77\n" \
    "v_cvt_pk_bf16_f32 v145, v74, v76\n" \
    "v_add_f32 v82, v82, v83\n" \
    "v_add_f32 v84, v84, v85\n" \
    "v_cvt_pk_bf16_f32 v147, v82, v84\n" \
    "v_permlane32_swap_b32 v144, v146\n" \
    "s_nop 0\n" \
    "v_permlane32_swap_b32 v145, v147\n" \
    "s_nop 1\n" \
    "v_permlane16_swap_b32 v144, v145\n" \
    "v_permlane16_swap_b32 v146, v147\n" \
    "s_cmp_eq_u32 s69, 0\n" \
    "s_cbranch_scc1 LnoVMW2_%=\n" \
    "buffer_store_dwordx4 v[144:147], v0, s[52:55], s65 offen sc1\n" \
    "LnoVMW2_%=:\n" \
    "s_memtime s[78:79]\n" \
    "s_waitcnt lgkmcnt(0)\n" \
    "s_sub_u32 s67, s78, s76\n" \
    "s_sub_i32 s67, 790, s67\n" \
    "s_cmp_lt_i32 s67, 36\n" \
    "s_cbranch_scc1 Ltg0W2_%=\n" \
    "s_mul_i32 s67, s67, 29\n" \
    "s_lshr_b32 s67, s67, 10\n" \
    "Ltl0W2_%=:\n" \
    "s_sub_u32 s67, s67, 1\n" \
    "s_cmp_lg_u32 s67, 0\n" \
    "s_cbranch_scc1 Ltl0W2_%=\n" \
    "Ltg0W2_%=:\n" \
    "buffer_load_dwordx4 v[134:137], v13, s[36:39], s61 offen sc1\n" \
    "s_nop 15\n" \
    "s_nop 15\n" \
    "buffer_load_dwordx4 v[102:105], v11, s[36:39], s61 offen sc1\n" \
    "buffer_load_dwordx4 v[110:113], v11, s[36:39], s61 offen offset:1024 sc1\n" \
    "buffer_load_dwordx2 v[142:143], v12, s[36:39], s61 offen sc1\n" \
    "s_nop 15\n" \
    "s_nop 15\n" \
    "s_nop 14\n" \
    "buffer_load_dwordx4 v[134:137], v13, s[36:39], s61 offen sc1\n" \
    "s_cmp_eq_u32 s60, 63\n" \
    "s_cbranch_scc1 LskipW2_%=\n" \
    "s_waitcnt vmcnt(4)\n" \
    "v_max3_u32 v248, v134, v135, v136\n" \
    "v_max_u32 v248, v248, v137\n" \
    "v_cmp_eq_u32 vcc, -1, v248\n" \
    "s_cbranch_vccz LokPW2_%=\n" \
    "s_add_u32 s84, s84, 0x100000\n" \
    "s_waitcnt vmcnt(0)\n" \
    "v_max3_u32 v248, v134, v135, v136\n" \
    "v_max_u32 v248, v248, v137\n" \
    "v_cmp_eq_u32 vcc, -1, v248\n" \
    "s_cbranch_vccz LokPW2_%=\n" \
    "s_add_u32 s84, s84, 0x100000\n" \
    "s_branch LslowPW2_%=\n" \
    "LokPW2_%=:\n" \
    "s_nop 0\n" \
    "v_cndmask_b32 v62, v136, v134, s[58:59]\n" \
    "v_cndmask_b32 v63, v137, v135, s[58:59]\n" \
    "v_cndmask_b32 v64, v134, v136, s[58:59]\n" \
    "v_cndmask_b32 v65, v135, v137, s[58:59]\n" \
    "v_mov_b32_dpp v62, v62 row_ror:8 row_mask:0xf bank_mask:0xf bound_ctrl:1\n" \
    "v_mov_b32_dpp v63, v63 row_ror:8 row_mask:0xf bank_mask:0xf bound_ctrl:1\n" \
    "v_lshlrev_b32 v54, 16, v64\n" \
    "v_and_b32 v55, 0xffff0000, v64\n" \
    "v_lshlrev_b32 v56, 16, v65\n" \
    "v_and_b32 v57, 0xffff0000, v65\n" \
    "v_lshlrev_b32 v58, 16, v62\n" \
    "v_and_b32 v59, 0xffff0000, v62\n" \
    "v_lshlrev_b32 v60, 16, v63\n" \
    "v_and_b32 v61, 0xffff0000, v63\n" \
    "v_pk_add_f32 v[54:55], v[54:55], v[58:59]\n" \
    "v_pk_add_f32 v[56:57], v[56:57], v[60:61]\n" \
    "v_max_f32 v66, v54, v55\n" \
    "v_max3_f32 v66, v66, v56, v57\n" \
    "s_nop 1\n" \
    "v_max_f32_dpp v66, v66, v66 row_ror:8 row_mask:0xf bank_mask:0xf\n" \
    "v_pk_add_f32 v[62:63], v[54:55], v[66:67] op_sel_hi:[1,0] neg_lo:[0,1] neg_hi:[0,1]\n" \
    "v_pk_add_f32 v[64:65], v[56:57], v[66:67] op_sel_hi:[1,0] neg_lo:[0,1] neg_hi:[0,1]\n" \
    "v_exp_f32 v62, v62\n" \
    "v_exp_f32 v63, v63\n" \
    "v_exp_f32 v64, v64\n" \
    "v_exp_f32 v65, v65\n" \
    "s_nop 0\n" \
    "v_pk_add_f32 v[248:249], v[62:63], v[64:65]\n" \
    "v_add_f32 v68, v248, v249\n" \
    "s_nop 1\n" \
    "v_add_f32_dpp v68, v68, v68 row_ror:8 row_mask:0xf bank_mask:0xf\n" \
    "s_waitcnt vmcnt(1)\n" \
    "v_max3_u32 v248, v102, v103, v104\n" \
    "v_max3_u32 v249, v105, v110, v111\n" \
    "v_max3_u32 v248, v248, v112, v113\n" \
    "v_max3_u32 v249, v249, v142, v143\n" \
    "v_max_u32 v248, v248, v249\n" \
    "v_cmp_eq_u32 vcc, -1, v248\n" \
    "s_cbranch_vccz LokEW2_%=\n" \
    "s_add_u32 s84, s84, 0x400\n" \
    "s_branch LslowEW2_%=\n" \
    "LokEW2_%=:\n" \
    "s_nop 0\n" \
    "v_mov_b32_dpp v106, v102 row_ror:8 row_mask:0xf bank_mask:0xf bound_ctrl:1\n" \
    "v_mov_b32_dpp v107, v103 row_ror:8 row_mask:0xf bank_mask:0xf bound_ctrl:1\n" \
    "v_mov_b32_dpp v108, v104 row_ror:8 row_mask:0xf bank_mask:0xf bound_ctrl:1\n" \
    "v_mov_b32_dpp v109, v105 row_ror:8 row_mask:0xf bank_mask:0xf bound_ctrl:1\n" \
    "v_mfma_f32_16x16x32_bf16 v[118:121], v[86:89], v[102:105], 0\n" \
    "v_max_f32 v58, v66, v142\n" \
    "v_mfma_f32_16x16x32_bf16 v[126:129], v[94:97], v[110:113], 0\n" \
    "v_sub_f32 v59, v66, v58\n" \
    "v_sub_f32 v60, v142, v58\n" \
    "v_mfma_f32_16x16x32_bf16 v[122:125], v[90:93], v[106:109], 0\n" \
    "v_exp_f32 v59, v59\n" \
    "v_exp_f32 v60, v60\n" \
    "v_mov_b32_dpp v114, v110 row_ror:8 row_mask:0xf bank_mask:0xf bound_ctrl:1\n" \
    "v_mov_b32_dpp v115, v111 row_ror:8 row_mask:0xf bank_mask:0xf bound_ctrl:1\n" \
    "v_mov_b32_dpp v116, v112 row_ror:8 row_mask:0xf bank_mask:0xf bound_ctrl:1\n" \
    "v_mov_b32_dpp v117, v113 row_ror:8 row_mask:0xf bank_mask:0xf bound_ctrl:1\n" \
    "v_mul_f32 v61, v59, v68\n" \
    "s_nop 0\n" \
    "v_mfma_f32_16x16x32_bf16 v[130:133], v[98:101], v[114:117], 0\n" \
    "v_fmac_f32 v61, v60, v143\n" \
    "v_rcp_f32 v61, v61\n" \
    "s_nop 0\n" \
    "v_mul_f32 v66, v59, v61\n" \
    "v_mul_f32 v250, v60, v61\n" \
    "v_pk_mul_f32 v[62:63], v[62:63], v[66:67] op_sel_hi:[1,0]\n" \
    "v_pk_mul_f32 v[64:65], v[64:65], v[66:67] op_sel_hi:[1,0]\n" \
    "v_cvt_pk_bf16_f32 v148, v62, v63\n" \
    "v_cvt_pk_bf16_f32 v149, v64, v65\n" \
    "v_mov_b32 v54, v250\n" \
    "v_mov_b32_dpp v150, v148 row_ror:8 row_mask:0xf bank_mask:0xf bound_ctrl:1\n" \
    "v_mov_b32_dpp v151, v149 row_ror:8 row_mask:0xf bank_mask:0xf bound_ctrl:1\n" \
    "v_permlane16_swap_b32 v250, v54\n" \
    "s_nop 0\n" \
    "v_mfma_f32_16x16x32_bf16 v[62:65], v[144:147], v[148:151], 0\n" \
    "v_mov_b32 v58, v250\n" \
    "v_mov_b32 v56, v54\n" \
    "s_nop 0\n" \
    "v_permlane32_swap_b32 v250, v58\n" \
    "v_permlane32_swap_b32 v54, v56\n" \
    "v_pk_mul_f32 v[138:139], v[118:119], v[250:251] op_sel_hi:[1,0]\n" \
    "v_pk_mul_f32 v[140:141], v[120:121], v[250:251] op_sel_hi:[1,0]\n" \
    "v_pk_fma_f32 v[138:139], v[126:127], v[58:59], v[138:139] op_sel_hi:[1,0,1]\n" \
    "v_pk_fma_f32 v[140:141], v[128:129], v[58:59], v[140:141] op_sel_hi:[1,0,1]\n" \
    "v_pk_fma_f32 v[138:139], v[122:123], v[54:55], v[138:139] op_sel_hi:[1,0,1]\n" \
    "v_pk_fma_f32 v[140:141], v[124:125], v[54:55], v[140:141] op_sel_hi:[1,0,1]\n" \
    "v_pk_fma_f32 v[138:139], v[130:131], v[56:57], v[138:139] op_sel_hi:[1,0,1]\n" \
    "v_pk_fma_f32 v[140:141], v[132:133], v[56:57], v[140:141] op_sel_hi:[1,0,1]\n" \
    "v_pk_add_f32 v[138:139], v[138:139], v[62:63]\n" \
    "v_pk_add_f32 v[140:141], v[140:141], v[64:65]\n" \
    "s_mov_b64 exec, s[74:75]\n" \
    "ds_write_b128 v14, v[138:141]\n" \
    "s_mov_b64 exec, -1\n" \
    "LskipW2_%=:\n" \
    "s_waitcnt lgkmcnt(0)\n" \
    "s_barrier\n" \
    "s_add_u32 s60, s60, 1\n" \
    "s_mov_b32 s61, s62\n" \
    "s_add_u32 s67, s60, 1\n" \
    "s_lshr_b32 s68, s67, 5\n" \
    "s_lshl_b32 s68, s68, 4\n" \
    "s_lshr_b32 s68, s72, s68\n" \
    "s_and_b32 s68, s68, 0x2000\n" \
    "s_lshl_b32 s67, s67, 16\n" \
    "s_add_u32 s62, s67, s68\n" \
    "s_add_u32 s63, s63, 458752\n" \
    "s_add_u32 s64, s64, 131072\n" \
    "s_add_u32 s65, s65, 114688\n" \
    "s_cmp_ge_u32 s60, s70\n" \
    "s_cbranch_scc1 LendW2_%=\n" \
    "v_readfirstlane_b32 s67, v254\n" \
    "s_nop 1\n" \
    "s_cmp_lt_u32 s67, 160\n" \
    "s_cbranch_scc1 LslowTW2_%=\n" \
    "LbackTW2_%=:\n" \
    "s_mov_b32 s67, s66\n" \
    "s_cmp_eq_u32 s67, 0\n" \
    "s_cbranch_scc1 LdAW2_%=_e\n" \
    "LdAW2_%=:\n" \
    "s_sub_u32 s67, s67, 1\n" \
    "s_cmp_lg_u32 s67, 0\n" \
    "s_cbranch_scc1 LdAW2_%=\n" \
    "LdAW2_%=_e:\n" \
    "s_branch LloopW2_%=\n" \
    "LslowMW2_%=:\n" \
    "s_mov_b32 s87, 0\n" \
    "buffer_load_dwordx4 v[26:29], v3, s[36:39], s61 offen sc1\n" \
    "buffer_load_dwordx4 v[30:33], v4, s[36:39], s61 offen sc1\n" \
    "s_nop 15\n" \
    "s_nop 15\n" \
    "s_nop 7\n" \
    "LslowMlW2_%=:\n" \
    "s_add_u32 s84, s84, 1\n" \
    "buffer_load_dwordx4 v[26:29], v3, s[36:39], s61 offen sc1\n" \
    "buffer_load_dwordx4 v[30:33], v4, s[36:39], s61 offen sc1\n" \
    "s_waitcnt vmcnt(2)\n" \
    "v_max3_u32 v248, v26, v27, v28\n" \
    "v_max3_u32 v249, v29, v30, v31\n" \
    "v_max3_u32 v248, v248, v32, v33\n" \
    "v_max_u32 v248, v248, v249\n" \
    "v_cmp_eq_u32 vcc, -1, v248\n" \
    "s_cbranch_vccz LokMW2_%=\n" \
    "s_add_u32 s87, s87, 1\n" \
    "s_cmp_lt_u32 s87, 0x8000\n" \
    "s_cbranch_scc1 LslowMlW2_%=\n" \
    "s_branch LokMW2_%=\n" \
    "LslowEW2_%=:\n" \
    "s_mov_b32 s87, 0\n" \
    "buffer_load_dwordx4 v[102:105], v11, s[36:39], s61 offen sc1\n" \
    "buffer_load_dwordx4 v[110:113], v11, s[36:39], s61 offen offset:1024 sc1\n" \
    "buffer_load_dwordx2 v[142:143], v12, s[36:39], s61 offen sc1\n" \
    "s_nop 15\n" \
    "s_nop 15\n" \
    "s_nop 7\n" \
    "LslowElW2_%=:\n" \
    "s_add_u32 s84, s84, 1024\n" \
    "buffer_load_dwordx4 v[102:105], v11, s[36:39], s61 offen sc1\n" \
    "buffer_load_dwordx4 v[110:113], v11, s[36:39], s61 offen offset:1024 sc1\n" \
    "buffer_load_dwordx2 v[142:143], v12, s[36:39], s61 offen sc1\n" \
    "s_waitcnt vmcnt(3)\n" \
    "v_max3_u32 v248, v102, v103, v104\n" \
    "v_max3_u32 v249, v105, v110, v111\n" \
    "v_max3_u32 v248, v248, v112, v113\n" \
    "v_max3_u32 v249, v249, v142, v143\n" \
    "v_max_u32 v248, v248, v249\n" \
    "v_cmp_eq_u32 vcc, -1, v248\n" \
    "s_cbranch_vccz LokEW2_%=\n" \
    "s_add_u32 s87, s87, 1\n" \
    "s_cmp_lt_u32 s87, 0x8000\n" \
    "s_cbranch_scc1 LslowElW2_%=\n" \
    "s_branch LokEW2_%=\n" \
    "LslowPW2_%=:\n" \
    "s_mov_b32 s87, 0\n" \
    "buffer_load_dwordx4 v[134:137], v13, s[36:39], s61 offen sc1\n" \
    "s_nop 15\n" \
    "s_nop 15\n" \
    "s_nop 7\n" \
    "LslowPlW2_%=:\n" \
    "s_add_u32 s84, s84, 1048576\n" \
    "buffer_load_dwordx4 v[134:137], v13, s[36:39], s61 offen sc1\n" \
    "s_waitcnt vmcnt(1)\n" \
    "v_max3_u32 v248, v134, v135, v136\n" \
    "v_max_u32 v248, v248, v137\n" \
    "v_cmp_eq_u32 vcc, -1, v248\n" \
    "s_cbranch_vccz LokPW2_%=\n" \
    "s_add_u32 s87, s87, 1\n" \
    "s_cmp_lt_u32 s87, 0x8000\n" \
    "s_cbranch_scc1 LslowPlW2_%=\n" \
    "s_branch LokPW2_%=\n" \
    "LslowTW2_%=:\n" \
    "s_mov_b32 s87, 0\n" \
    "s_lshr_b32 s68, s60, 2\n" \
    "s_lshl_b32 s68, s68, 8\n" \
    "v_mov_b32 v253, s68\n" \
    "LslowTlW2_%=:\n" \
    "global_load_dword v254, v253, s[40:41] sc1\n" \
    "s_waitcnt vmcnt(0)\n" \
    "v_readfirstlane_b32 s67, v254\n" \
    "s_nop 1\n" \
    "s_cmp_ge_u32 s67, 160\n" \
    "s_cbranch_scc1 LbackTW2_%=\n" \
    "s_sleep 2\n" \
    "s_add_u32 s87, s87, 1\n" \
    "s_cmp_lt_u32 s87, 0x40000\n" \
    "s_cbranch_scc1 LslowTlW2_%=\n" \
    "s_branch LbackTW2_%=\n" \
    "LendW2_%=:\n" \
    "s_waitcnt vmcnt(0)\n" \
    "s_barrier\n" \
    "s_branch Ldone_%=\n" \
    "LwW3_%=:\n" \
    "v_add_u32 v248, 86016, v248\n" \
    "ds_read_b32 v0, v248\n" \
    "ds_read_b32 v1, v248 offset:256\n" \
    "ds_read_b32 v2, v248 offset:512\n" \
    "ds_read_b32 v3, v248 offset:768\n" \
    "ds_read_b32 v4, v248 offset:1024\n" \
    "ds_read_b32 v5, v248 offset:1280\n" \
    "ds_read_b32 v6, v248 offset:1536\n" \
    "ds_read_b32 v7, v248 offset:1792\n" \
    "s_waitcnt lgkmcnt(0)\n" \
    "ds_read_b32 v8, v248 offset:2048\n" \
    "ds_read_b32 v9, v248 offset:2304\n" \
    "ds_read_b32 v10, v248 offset:2560\n" \
    "ds_read_b32 v11, v248 offset:2816\n" \
    "ds_read_b32 v12, v248 offset:3072\n" \
    "ds_read_b32 v13, v248 offset:3328\n" \
    "ds_read_b32 v14, v248 offset:3584\n" \
    "ds_read_b32 v15, v248 offset:3840\n" \
    "s_waitcnt lgkmcnt(0)\n" \
    "ds_read_b32 v16, v248 offset:4096\n" \
    "ds_read_b32 v17, v248 offset:4352\n" \
    "ds_read_b32 v18, v248 offset:4608\n" \
    "ds_read_b32 v19, v248 offset:4864\n" \
    "ds_read_b32 v20, v248 offset:5120\n" \
    "ds_read_b32 v21, v248 offset:5376\n" \
    "ds_read_b32 v22, v248 offset:5632\n" \
    "ds_read_b32 v23, v248 offset:5888\n" \
    "s_waitcnt lgkmcnt(0)\n" \
    "ds_read_b32 v24, v248 offset:6144\n" \
    "s_waitcnt lgkmcnt(0)\n" \
    "s_add_u32 s67, s85, 176128\n" \
    "buffer_load_dwordx4 a[0:3], v21, s[88:91], s67 offen\n" \
    "s_add_u32 s67, s85, 204800\n" \
    "buffer_load_dwordx4 a[4:7], v21, s[88:91], s67 offen\n" \
    "s_add_u32 s67, s85, 4096\n" \
    "buffer_load_dwordx4 a[8:11], v21, s[88:91], s67 offen\n" \
    "s_add_u32 s67, s85, 32768\n" \
    "buffer_load_dwordx4 a[12:15], v21, s[88:91], s67 offen\n" \
    "s_add_u32 s67, s85, 61440\n" \
    "buffer_load_dwordx4 a[16:19], v21, s[88:91], s67 offen\n" \
    "s_add_u32 s67, s85, 90112\n" \
    "buffer_load_dwordx4 a[20:23], v21, s[88:91], s67 offen\n" \
    "s_add_u32 s67, s85, 118784\n" \
    "buffer_load_dwordx4 a[24:27], v21, s[88:91], s67 offen\n" \
    "s_add_u32 s67, s85, 147456\n" \
    "buffer_load_dwordx4 a[28:31], v21, s[88:91], s67 offen\n" \
    "s_add_u32 s67, s85, 233472\n" \
    "buffer_load_dwordx4 a[32:35], v21, s[88:91], s67 offen\n" \
    "s_add_u32 s67, s85, 262144\n" \
    "buffer_load_dwordx4 a[36:39], v21, s[88:91], s67 offen\n" \
    "s_add_u32 s67, s85, 290816\n" \
    "buffer_load_dwordx4 a[40:43], v21, s[88:91], s67 offen\n" \
    "s_add_u32 s67, s85, 319488\n" \
    "buffer_load_dwordx4 a[44:47], v21, s[88:91], s67 offen\n" \
    "s_add_u32 s67, s85, 348160\n" \
    "buffer_load_dwordx4 a[48:51], v21, s[88:91], s67 offen\n" \
    "s_add_u32 s67, s85, 376832\n" \
    "buffer_load_dwordx4 a[52:55], v21, s[88:91], s67 offen\n" \
    "s_add_u32 s67, s85, 177152\n" \
    "buffer_load_dwordx4 a[56:59], v20, s[88:91], s67 offen\n" \
    "s_add_u32 s67, s85, 205824\n" \
    "buffer_load_dwordx4 a[60:63], v20, s[88:91], s67 offen\n" \
    "s_add_u32 s67, s85, 5120\n" \
    "buffer_load_dwordx4 a[64:67], v20, s[88:91], s67 offen\n" \
    "s_add_u32 s67, s85, 33792\n" \
    "buffer_load_dwordx4 a[68:71], v20, s[88:91], s67 offen\n" \
    "s_add_u32 s67, s85, 62464\n" \
    "buffer_load_dwordx4 a[72:75], v20, s[88:91], s67 offen\n" \
    "s_add_u32 s67, s85, 91136\n" \
    "buffer_load_dwordx4 a[76:79], v20, s[88:91], s67 offen\n" \
    "s_add_u32 s67, s85, 119808\n" \
    "buffer_load_dwordx4 a[80:83], v20, s[88:91], s67 offen\n" \
    "s_add_u32 s67, s85, 148480\n" \
    "buffer_load_dwordx4 a[84:87], v20, s[88:91], s67 offen\n" \
    "s_add_u32 s67, s85, 234496\n" \
    "buffer_load_dwordx4 a[88:91], v20, s[88:91], s67 offen\n" \
    "s_add_u32 s67, s85, 263168\n" \
    "buffer_load_dwordx4 a[92:95], v20, s[88:91], s67 offen\n" \
    "s_add_u32 s67, s85, 291840\n" \
    "buffer_load_dwordx4 a[96:99], v20, s[88:91], s67 offen\n" \
    "s_add_u32 s67, s85, 320512\n" \
    "buffer_load_dwordx4 a[100:103], v20, s[88:91], s67 offen\n" \
    "s_add_u32 s67, s85, 349184\n" \
    "buffer_load_dwordx4 a[104:107], v20, s[88:91], s67 offen\n" \
    "s_add_u32 s67, s85, 377856\n" \
    "buffer_load_dwordx4 a[108:111], v20, s[88:91], s67 offen\n" \
    "s_add_u32 s67, s85, 177152\n" \
    "buffer_load_dwordx4 a[112:115], v21, s[88:91], s67 offen\n" \
    "s_add_u32 s67, s85, 205824\n" \
    "buffer_load_dwordx4 a[116:119], v21, s[88:91], s67 offen\n" \
    "s_add_u32 s67, s85, 5120\n" \
    "buffer_load_dwordx4 a[120:123], v21, s[88:91], s67 offen\n" \
    "s_add_u32 s67, s85, 33792\n" \
    "buffer_load_dwordx4 a[124:127], v21, s[88:91], s67 offen\n" \
    "s_add_u32 s67, s85, 62464\n" \
    "buffer_load_dwordx4 a[128:131], v21, s[88:91], s67 offen\n" \
    "s_add_u32 s67, s85, 91136\n" \
    "buffer_load_dwordx4 a[132:135], v21, s[88:91], s67 offen\n" \
    "s_add_u32 s67, s85, 119808\n" \
    "buffer_load_dwordx4 a[136:139], v21, s[88:91], s67 offen\n" \
    "s_add_u32 s67, s85, 148480\n" \
    "buffer_load_dwordx4 a[140:143], v21, s[88:91], s67 offen\n" \
    "s_add_u32 s67, s85, 234496\n" \
    "buffer_load_dwordx4 a[144:147], v21, s[88:91], s67 offen\n" \
    "s_add_u32 s67, s85, 263168\n" \
    "buffer_load_dwordx4 a[148:151], v21, s[88:91], s67 offen\n" \
    "s_add_u32 s67, s85, 291840\n" \
    "buffer_load_dwordx4 a[152:155], v21, s[88:91], s67 offen\n" \
    "s_add_u32 s67, s85, 320512\n" \
    "buffer_load_dwordx4 a[156:159], v21, s[88:91], s67 offen\n" \
    "s_add_u32 s67, s85, 349184\n" \
    "buffer_load_dwordx4 a[160:163], v21, s[88:91], s67 offen\n" \
    "s_add_u32 s67, s85, 377856\n" \
    "buffer_load_dwordx4 a[164:167], v21, s[88:91], s67 offen\n" \
    "s_add_u32 s67, s85, 235520\n" \
    "buffer_load_dwordx4 a[168:171], v21, s[88:91], s67 offen\n" \
    "s_add_u32 s67, s85, 264192\n" \
    "buffer_load_dwordx4 a[172:175], v21, s[88:91], s67 offen\n" \
    "s_add_u32 s67, s85, 292864\n" \
    "buffer_load_dwordx4 a[176:179], v21, s[88:91], s67 offen\n" \
    "s_add_u32 s67, s85, 321536\n" \
    "buffer_load_dwordx4 a[180:183], v21, s[88:91], s67 offen\n" \
    "s_add_u32 s67, s85, 350208\n" \
    "buffer_load_dwordx4 a[184:187], v21, s[88:91], s67 offen\n" \
    "s_add_u32 s67, s85, 378880\n" \
    "buffer_load_dwordx4 a[188:191], v21, s[88:91], s67 offen\n" \
    "s_waitcnt vmcnt(0)\n" \
    "s_add_u32 s67, s86, 4988928\n" \
    "buffer_load_dwordx4 a[200:203], v22, s[92:95], s67 offen\n" \
    "s_add_u32 s67, s86, 5017600\n" \
    "buffer_load_dwordx4 a[204:207], v22, s[92:95], s67 offen\n" \
    "s_add_u32 s67, s86, 4816896\n" \
    "buffer_load_dwordx4 a[208:211], v22, s[92:95], s67 offen\n" \
    "s_add_u32 s67, s86, 4845568\n" \
    "buffer_load_dwordx4 a[212:215], v22, s[92:95], s67 offen\n" \
    "s_add_u32 s67, s86, 4874240\n" \
    "buffer_load_dwordx4 a[216:219], v22, s[92:95], s67 offen\n" \
    "s_add_u32 s67, s86, 4902912\n" \
    "buffer_load_dwordx4 a[220:223], v22, s[92:95], s67 offen\n" \
    "s_add_u32 s67, s86, 4931584\n" \
    "buffer_load_dwordx4 a[224:227], v22, s[92:95], s67 offen\n" \
    "s_add_u32 s67, s86, 4960256\n" \
    "buffer_load_dwordx4 a[228:231], v22, s[92:95], s67 offen\n" \
    "s_add_u32 s67, s86, 5046272\n" \
    "buffer_load_dwordx4 a[232:235], v22, s[92:95], s67 offen\n" \
    "s_add_u32 s67, s86, 5074944\n" \
    "buffer_load_dwordx4 a[236:239], v22, s[92:95], s67 offen\n" \
    "s_add_u32 s67, s86, 5103616\n" \
    "buffer_load_dwordx4 a[240:243], v22, s[92:95], s67 offen\n" \
    "s_add_u32 s67, s86, 5132288\n" \
    "buffer_load_dwordx4 a[244:247], v22, s[92:95], s67 offen\n" \
    "s_add_u32 s67, s86, 5160960\n" \
    "buffer_load_dwordx4 a[248:251], v22, s[92:95], s67 offen\n" \
    "s_add_u32 s67, s86, 5189632\n" \
    "buffer_load_dwordx4 a[252:255], v22, s[92:95], s67 offen\n" \
    "s_add_u32 s67, s86, 5390336\n" \
    "buffer_load_dwordx4 v[192:195], v22, s[92:95], s67 offen\n" \
    "s_add_u32 s67, s86, 5419008\n" \
    "buffer_load_dwordx4 v[196:199], v22, s[92:95], s67 offen\n" \
    "s_add_u32 s67, s86, 5218304\n" \
    "buffer_load_dwordx4 v[200:203], v22, s[92:95], s67 offen\n" \
    "s_add_u32 s67, s86, 5246976\n" \
    "buffer_load_dwordx4 v[204:207], v22, s[92:95], s67 offen\n" \
    "s_add_u32 s67, s86, 5275648\n" \
    "buffer_load_dwordx4 v[208:211], v22, s[92:95], s67 offen\n" \
    "s_add_u32 s67, s86, 5304320\n" \
    "buffer_load_dwordx4 v[212:215], v22, s[92:95], s67 offen\n" \
    "s_add_u32 s67, s86, 5332992\n" \
    "buffer_load_dwordx4 v[216:219], v22, s[92:95], s67 offen\n" \
    "s_add_u32 s67, s86, 5361664\n" \
    "buffer_load_dwordx4 v[220:223], v22, s[92:95], s67 offen\n" \
    "s_add_u32 s67, s86, 5447680\n" \
    "buffer_load_dwordx4 v[224:227], v22, s[92:95], s67 offen\n" \
    "s_add_u32 s67, s86, 5476352\n" \
    "buffer_load_dwordx4 v[228:231], v22, s[92:95], s67 offen\n" \
    "s_add_u32 s67, s86, 5505024\n" \
    "buffer_load_dwordx4 v[232:235], v22, s[92:95], s67 offen\n" \
    "s_add_u32 s67, s86, 5533696\n" \
    "buffer_load_dwordx4 v[236:239], v22, s[92:95], s67 offen\n" \
    "s_add_u32 s67, s86, 5562368\n" \
    "buffer_load_dwordx4 v[240:243], v22, s[92:95], s67 offen\n" \
    "s_add_u32 s67, s86, 5591040\n" \
    "buffer_load_dwordx4 v[244:247], v22, s[92:95], s67 offen\n" \
    "s_add_u32 s67, s86, 5791744\n" \
    "buffer_load_dwordx4 v[152:155], v22, s[92:95], s67 offen\n" \
    "s_add_u32 s67, s86, 5820416\n" \
    "buffer_load_dwordx4 v[156:159], v22, s[92:95], s67 offen\n" \
    "s_add_u32 s67, s86, 5619712\n" \
    "buffer_load_dwordx4 v[160:163], v22, s[92:95], s67 offen\n" \
    "s_add_u32 s67, s86, 5648384\n" \
    "buffer_load_dwordx4 v[164:167], v22, s[92:95], s67 offen\n" \
    "s_add_u32 s67, s86, 5677056\n" \
    "buffer_load_dwordx4 v[168:171], v22, s[92:95], s67 offen\n" \
    "s_add_u32 s67, s86, 5705728\n" \
    "buffer_load_dwordx4 v[172:175], v22, s[92:95], s67 offen\n" \
    "s_add_u32 s67, s86, 5734400\n" \
    "buffer_load_dwordx4 v[176:179], v22, s[92:95], s67 offen\n" \
    "s_add_u32 s67, s86, 5763072\n" \
    "buffer_load_dwordx4 v[180:183], v22, s[92:95], s67 offen\n" \
    "s_add_u32 s67, s86, 5849088\n" \
    "buffer_load_dwordx4 v[184:187], v22, s[92:95], s67 offen\n" \
    "s_add_u32 s67, s86, 5877760\n" \
    "buffer_load_dwordx4 v[188:191], v22, s[92:95], s67 offen\n" \
    "s_waitcnt vmcnt(0)\n" \
    "ds_write_b128 v17, v[152:155]\n" \
    "ds_write_b128 v17, v[156:159] offset:1024\n" \
    "ds_write_b128 v17, v[160:163] offset:2048\n" \
    "ds_write_b128 v17, v[164:167] offset:3072\n" \
    "ds_write_b128 v17, v[168:171] offset:4096\n" \
    "ds_write_b128 v17, v[172:175] offset:5120\n" \
    "ds_write_b128 v17, v[176:179] offset:6144\n" \
    "ds_write_b128 v17, v[180:183] offset:7168\n" \
    "ds_write_b128 v17, v[184:187] offset:8192\n" \
    "ds_write_b128 v17, v[188:191] offset:9216\n" \
    "s_waitcnt lgkmcnt(0)\n" \
    "s_add_u32 s67, s86, 5906432\n" \
    "buffer_load_dwordx4 v[152:155], v22, s[92:95], s67 offen\n" \
    "s_add_u32 s67, s86, 5935104\n" \
    "buffer_load_dwordx4 v[156:159], v22, s[92:95], s67 offen\n" \
    "s_add_u32 s67, s86, 5963776\n" \
    "buffer_load_dwordx4 v[160:163], v22, s[92:95], s67 offen\n" \
    "s_add_u32 s67, s86, 5992448\n" \
    "buffer_load_dwordx4 v[164:167], v22, s[92:95], s67 offen\n" \
    "s_add_u32 s67, s86, 6193152\n" \
    "buffer_load_dwordx4 v[168:171], v22, s[92:95], s67 offen\n" \
    "s_add_u32 s67, s86, 6221824\n" \
    "buffer_load_dwordx4 v[172:175], v22, s[92:95], s67 offen\n" \
    "s_add_u32 s67, s86, 6021120\n" \
    "buffer_load_dwordx4 v[176:179], v22, s[92:95], s67 offen\n" \
    "s_add_u32 s67, s86, 6049792\n" \
    "buffer_load_dwordx4 v[180:183], v22, s[92:95], s67 offen\n" \
    "s_add_u32 s67, s86, 6078464\n" \
    "buffer_load_dwordx4 v[184:187], v22, s[92:95], s67 offen\n" \
    "s_add_u32 s67, s86, 6107136\n" \
    "buffer_load_dwordx4 v[188:191], v22, s[92:95], s67 offen\n" \
    "s_waitcnt vmcnt(0)\n" \
    "ds_write_b128 v17, v[152:155] offset:10240\n" \
    "ds_write_b128 v17, v[156:159] offset:11264\n" \
    "ds_write_b128 v17, v[160:163] offset:12288\n" \
    "ds_write_b128 v17, v[164:167] offset:13312\n" \
    "ds_write_b128 v17, v[168:171] offset:14336\n" \
    "ds_write_b128 v17, v[172:175] offset:15360\n" \
    "ds_write_b128 v17, v[176:179] offset:16384\n" \
    "ds_write_b128 v17, v[180:183] offset:17408\n" \
    "ds_write_b128 v17, v[184:187] offset:18432\n" \
    "ds_write_b128 v17, v[188:191] offset:19456\n" \
    "s_waitcnt lgkmcnt(0)\n" \
    "s_add_u32 s67, s86, 6135808\n" \
    "buffer_load_dwordx4 v[152:155], v22, s[92:95], s67 offen\n" \
    "s_add_u32 s67, s86, 6164480\n" \
    "buffer_load_dwordx4 v[156:159], v22, s[92:95], s67 offen\n" \
    "s_add_u32 s67, s86, 6250496\n" \
    "buffer_load_dwordx4 v[160:163], v22, s[92:95], s67 offen\n" \
    "s_add_u32 s67, s86, 6279168\n" \
    "buffer_load_dwordx4 v[164:167], v22, s[92:95], s67 offen\n" \
    "s_add_u32 s67, s86, 6307840\n" \
    "buffer_load_dwordx4 v[168:171], v22, s[92:95], s67 offen\n" \
    "s_add_u32 s67, s86, 6336512\n" \
    "buffer_load_dwordx4 v[172:175], v22, s[92:95], s67 offen\n" \
    "s_add_u32 s67, s86, 6365184\n" \
    "buffer_load_dwordx4 v[176:179], v22, s[92:95], s67 offen\n" \
    "s_add_u32 s67, s86, 6393856\n" \
    "buffer_load_dwordx4 v[180:183], v22, s[92:95], s67 offen\n" \
    "s_waitcnt vmcnt(0)\n" \
    "ds_write_b128 v17, v[152:155] offset:20480\n" \
    "ds_write_b128 v17, v[156:159] offset:21504\n" \
    "ds_write_b128 v17, v[160:163] offset:22528\n" \
    "ds_write_b128 v17, v[164:167] offset:23552\n" \
    "ds_write_b128 v17, v[168:171] offset:24576\n" \
    "ds_write_b128 v17, v[172:175] offset:25600\n" \
    "ds_write_b128 v17, v[176:179] offset:26624\n" \
    "ds_write_b128 v17, v[180:183] offset:27648\n" \
    "s_waitcnt lgkmcnt(0)\n" \
    "s_mov_b32 s87, 0\n" \
    "Lt0W3_%=:\n" \
    "v_mov_b32 v248, 0\n" \
    "global_load_dword v254, v248, s[40:41] sc1\n" \
    "s_waitcnt vmcnt(0)\n" \
    "v_readfirstlane_b32 s67, v254\n" \
    "s_nop 1\n" \
    "s_cmp_ge_u32 s67, 160\n" \
    "s_cbranch_scc1 Lt0dW3_%=\n" \
    "s_sleep 4\n" \
    "s_add_u32 s87, s87, 1\n" \
    "s_cmp_lt_u32 s87, 0x40000\n" \
    "s_cbranch_scc1 Lt0W3_%=\n" \
    "Lt0dW3_%=:\n" \
    "s_mov_b32 s87, 0\n" \
    "Lp0W3_%=:\n" \
    "v_mov_b32 v248, 45120\n" \
    "global_load_dword v254, v248, s[40:41] sc1\n" \
    "s_waitcnt vmcnt(0)\n" \
    "v_readfirstlane_b32 s72, v254\n" \
    "s_nop 1\n" \
    "s_cmp_lg_u32 s72, -1\n" \
    "s_cbranch_scc1 Lp0dW3_%=\n" \
    "s_add_u32 s87, s87, 1\n" \
    "s_cmp_lt_u32 s87, 0x100000\n" \
    "s_cbranch_scc1 Lp0W3_%=\n" \
    "s_mov_b32 s72, 0\n" \
    "Lp0dW3_%=:\n" \
    "s_and_b32 s61, s72, 0x2000\n" \
    "s_add_u32 s62, s61, 65536\n" \
    "s_waitcnt vmcnt(0)\n" \
    "s_waitcnt lgkmcnt(0)\n" \
    "s_barrier\n" \
    "s_memrealtime s[96:97]\n" \
    "s_waitcnt lgkmcnt(0)\n" \
    "LloopW3_%=:\n" \
    "buffer_load_dwordx4 v[26:29], v3, s[36:39], s61 offen sc1\n" \
    "buffer_load_dwordx4 v[30:33], v4, s[36:39], s61 offen sc1\n" \
    "v_mov_b32 v54, 0\n" \
    "v_mov_b32 v55, 0\n" \
    "v_mov_b32 v56, 0\n" \
    "v_mov_b32 v57, 0\n" \
    "v_mov_b32 v58, 0\n" \
    "v_mov_b32 v59, 0\n" \
    "v_mov_b32 v60, 0\n" \
    "v_mov_b32 v61, 0\n" \
    "v_mov_b32 v62, 0\n" \
    "v_mov_b32 v63, 0\n" \
    "v_mov_b32 v64, 0\n" \
    "v_mov_b32 v65, 0\n" \
    "v_mov_b32 v66, 0\n" \
    "v_mov_b32 v67, 0\n" \
    "v_mov_b32 v68, 0\n" \
    "v_mov_b32 v69, 0\n" \
    "v_mov_b32 v70, 0\n" \
    "v_mov_b32 v71, 0\n" \
    "v_mov_b32 v72, 0\n" \
    "v_mov_b32 v73, 0\n" \
    "v_mov_b32 v74, 0\n" \
    "v_mov_b32 v75, 0\n" \
    "v_mov_b32 v76, 0\n" \
    "v_mov_b32 v77, 0\n" \
    "v_mov_b32 v78, 0\n" \
    "v_mov_b32 v79, 0\n" \
    "v_mov_b32 v80, 0\n" \
    "v_mov_b32 v81, 0\n" \
    "v_mov_b32 v82, 0\n" \
    "v_mov_b32 v83, 0\n" \
    "v_mov_b32 v84, 0\n" \
    "v_mov_b32 v85, 0\n" \
    "s_nop 15\n" \
    "s_nop 7\n" \
    "buffer_load_dwordx4 v[26:29], v3, s[36:39], s61 offen sc1\n" \
    "buffer_load_dwordx4 v[30:33], v4, s[36:39], s61 offen sc1\n" \
    "s_waitcnt vmcnt(2)\n" \
    "v_max3_u32 v248, v26, v27, v28\n" \
    "v_max3_u32 v249, v29, v30, v31\n" \
    "v_max3_u32 v248, v248, v32, v33\n" \
    "v_max_u32 v248, v248, v249\n" \
    "v_cmp_eq_u32 vcc, -1, v248\n" \
    "s_cbranch_vccz LokMW3_%=\n" \
    "s_add_u32 s84, s84, 1\n" \
    "s_waitcnt vmcnt(0)\n" \
    "v_max3_u32 v248, v26, v27, v28\n" \
    "v_max3_u32 v249, v29, v30, v31\n" \
    "v_max3_u32 v248, v248, v32, v33\n" \
    "v_max_u32 v248, v248, v249\n" \
    "v_cmp_eq_u32 vcc, -1, v248\n" \
    "s_cbranch_vccz LokMW3_%=\n" \
    "s_add_u32 s84, s84, 1\n" \
    "s_branch LslowMW3_%=\n" \
    "LokMW3_%=:\n" \
    "s_nop 0\n" \
    "ds_write_b128 v5, v[26:29] offset:3072\n" \
    "v_smfmac_f32_16x16x64_bf16 v[54:57], v[26:29], a[0:7], v1\n" \
    "v_smfmac_f32_16x16x64_bf16 v[58:61], v[26:29], a[56:63], v1\n" \
    "v_smfmac_f32_16x16x64_bf16 v[62:65], v[26:29], a[112:119], v1\n" \
    "s_waitcnt lgkmcnt(0)\n" \
    "s_barrier\n" \
    "ds_read_b128 v[34:37], v5 offset:1024\n" \
    "ds_read_b128 v[38:41], v5 offset:2048\n" \
    "ds_read_b128 v[42:45], v5 offset:4096\n" \
    "ds_read_b128 v[46:49], v5 offset:5120\n" \
    "ds_read_b128 v[50:53], v5 offset:6144\n" \
    "v_smfmac_f32_16x16x64_bf16 v[54:57], v[30:33], a[8:15], v1\n" \
    "v_smfmac_f32_16x16x64_bf16 v[58:61], v[30:33], a[64:71], v1\n" \
    "v_smfmac_f32_16x16x64_bf16 v[62:65], v[30:33], a[120:127], v1\n" \
    "s_waitcnt lgkmcnt(4)\n" \
    "v_smfmac_f32_16x16x64_bf16 v[54:57], v[34:37], a[16:23], v1\n" \
    "v_smfmac_f32_16x16x64_bf16 v[58:61], v[34:37], a[72:79], v1\n" \
    "buffer_load_dwordx4 v[86:89], v0, s[44:47], s63 offen sc1\n" \
    "v_smfmac_f32_16x16x64_bf16 v[62:65], v[34:37], a[128:135], v1\n" \
    "s_waitcnt lgkmcnt(2)\n" \
    "v_smfmac_f32_16x16x64_bf16 v[66:69], v[42:45], a[168:175], v1\n" \
    "s_waitcnt lgkmcnt(1)\n" \
    "v_smfmac_f32_16x16x64_bf16 v[66:69], v[46:49], a[176:183], v1\n" \
    "s_waitcnt lgkmcnt(0)\n" \
    "v_smfmac_f32_16x16x64_bf16 v[66:69], v[50:53], a[184:191], v1\n" \
    "buffer_load_dwordx4 v[90:93], v0, s[44:47], s63 offen offset:1024 sc1\n" \
    "v_smfmac_f32_16x16x64_bf16 v[54:57], v[38:41], a[24:31], v1\n" \
    "ds_read_b128 v[152:155], v17\n" \
    "ds_read_b128 v[156:159], v17 offset:1024\n" \
    "ds_read_b128 v[160:163], v17 offset:2048\n" \
    "v_smfmac_f32_16x16x64_bf16 v[54:57], v[42:45], a[32:39], v1\n" \
    "ds_read_b128 v[164:167], v17 offset:3072\n" \
    "ds_read_b128 v[168:171], v17 offset:4096\n" \
    "ds_read_b128 v[172:175], v17 offset:5120\n" \
    "v_smfmac_f32_16x16x64_bf16 v[54:57], v[46:49], a[40:47], v1\n" \
    "v_add_f32 v250, v66, v67\n" \
    "v_add_f32 v251, v68, v69\n" \
    "v_cvt_pk_bf16_f32 v250, v250, v251\n" \
    "v_smfmac_f32_16x16x64_bf16 v[54:57], v[50:53], a[48:55], v1\n" \
    "buffer_load_dwordx4 v[94:97], v0, s[44:47], s63 offen offset:2048 sc1\n" \
    "ds_write_b32 v7, v250\n" \
    "ds_read_b128 v[176:179], v17 offset:6144\n" \
    "ds_read_b128 v[180:183], v17 offset:7168\n" \
    "v_smfmac_f32_16x16x64_bf16 v[58:61], v[38:41], a[80:87], v1\n" \
    "ds_read_b128 v[184:187], v17 offset:8192\n" \
    "ds_read_b128 v[188:191], v17 offset:9216\n" \
    "v_smfmac_f32_16x16x64_bf16 v[58:61], v[42:45], a[88:95], v1\n" \
    "v_smfmac_f32_16x16x64_bf16 v[58:61], v[46:49], a[96:103], v1\n" \
    "v_add_f32 v248, v54, v55\n" \
    "v_add_f32 v249, v56, v57\n" \
    "v_cvt_pk_bf16_f32 v248, v248, v249\n" \
    "v_smfmac_f32_16x16x64_bf16 v[58:61], v[50:53], a[104:111], v1\n" \
    "buffer_load_dwordx4 v[98:101], v0, s[44:47], s63 offen offset:3072 sc1\n" \
    "ds_write_b32 v6, v248\n" \
    "v_smfmac_f32_16x16x64_bf16 v[62:65], v[38:41], a[136:143], v1\n" \
    "v_smfmac_f32_16x16x64_bf16 v[62:65], v[42:45], a[144:151], v1\n" \
    "v_smfmac_f32_16x16x64_bf16 v[62:65], v[46:49], a[152:159], v1\n" \
    "s_nop 2\n" \
    "v_add_f32 v250, v58, v59\n" \
    "v_add_f32 v251, v60, v61\n" \
    "v_cvt_pk_bf16_f32 v250, v250, v251\n" \
    "v_smfmac_f32_16x16x64_bf16 v[62:65], v[50:53], a[160:167], v1\n" \
    "ds_write_b32 v6, v250 offset:256\n" \
    "s_nop 6\n" \
    "v_add_f32 v248, v62, v63\n" \
    "v_add_f32 v249, v64, v65\n" \
    "v_cvt_pk_bf16_f32 v248, v248, v249\n" \
    "ds_write_b32 v6, v248 offset:512\n" \
    "s_waitcnt lgkmcnt(0)\n" \
    "s_barrier\n" \
    "s_memtime s[76:77]\n" \
    "s_waitcnt lgkmcnt(0)\n" \
    "ds_read_b64_tr_b16 v[118:119], v18\n" \
    "ds_read_b64_tr_b16 v[120:121], v18 offset:64\n" \
    "v_smfmac_f32_16x16x64_bf16 v[70:73], v[26:29], a[200:207], v1\n" \
    "v_smfmac_f32_16x16x64_bf16 v[78:81], v[26:29], v[152:159], v1\n" \
    "ds_read_b128 v[152:155], v17 offset:10240\n" \
    "ds_read_b128 v[156:159], v17 offset:11264\n" \
    "v_smfmac_f32_16x16x64_bf16 v[70:73], v[30:33], a[208:215], v1\n" \
    "v_smfmac_f32_16x16x64_bf16 v[78:81], v[30:33], v[160:167], v1\n" \
    "ds_read_b128 v[160:163], v17 offset:12288\n" \
    "ds_read_b128 v[164:167], v17 offset:13312\n" \
    "v_smfmac_f32_16x16x64_bf16 v[70:73], v[34:37], a[216:223], v1\n" \
    "v_smfmac_f32_16x16x64_bf16 v[78:81], v[34:37], v[168:175], v1\n" \
    "ds_read_b128 v[168:171], v17 offset:14336\n" \
    "ds_read_b128 v[172:175], v17 offset:15360\n" \
    "v_smfmac_f32_16x16x64_bf16 v[70:73], v[38:41], a[224:231], v1\n" \
    "v_smfmac_f32_16x16x64_bf16 v[78:81], v[38:41], v[176:183], v1\n" \
    "ds_read_b128 v[176:179], v17 offset:16384\n" \
    "ds_read_b128 v[180:183], v17 offset:17408\n" \
    "v_smfmac_f32_16x16x64_bf16 v[70:73], v[42:45], a[232:239], v1\n" \
    "v_smfmac_f32_16x16x64_bf16 v[78:81], v[42:45], v[184:191], v1\n" \
    "ds_read_b128 v[184:187], v17 offset:18432\n" \
    "ds_read_b128 v[188:191], v17 offset:19456\n" \
    "v_smfmac_f32_16x16x64_bf16 v[70:73], v[46:49], a[240:247], v1\n" \
    "s_cmp_eq_u32 s60, 0\n" \
    "s_cbranch_scc1 LncW3_%=\n" \
    "s_lshl_b32 s67, s60, 8\n" \
    "s_sub_u32 s67, s67, 256\n" \
    "v_mov_b32 v248, s67\n" \
    "v_mov_b32 v249, 1\n" \
    "s_mov_b64 exec, 1\n" \
    "global_atomic_add v248, v249, s[56:57] sc1\n" \
    "s_mov_b64 exec, -1\n" \
    "LncW3_%=:\n" \
    "s_waitcnt lgkmcnt(8)\n" \
    "v_smfmac_f32_16x16x64_bf16 v[78:81], v[46:49], v[152:159], v1\n" \
    "ds_read_b128 v[152:155], v17 offset:20480\n" \
    "ds_read_b128 v[156:159], v17 offset:21504\n" \
    "v_smfmac_f32_16x16x64_bf16 v[70:73], v[50:53], a[248:255], v1\n" \
    "s_waitcnt lgkmcnt(8)\n" \
    "v_smfmac_f32_16x16x64_bf16 v[78:81], v[50:53], v[160:167], v1\n" \
    "ds_read_b128 v[160:163], v17 offset:22528\n" \
    "ds_read_b128 v[164:167], v17 offset:23552\n" \
    "s_add_u32 s67, s60, 1\n" \
    "s_lshr_b32 s67, s67, 2\n" \
    "s_min_u32 s67, s67, 15\n" \
    "s_lshl_b32 s67, s67, 8\n" \
    "v_mov_b32 v253, s67\n" \
    "global_load_dword v254, v253, s[40:41] sc1\n" \
    "v_smfmac_f32_16x16x64_bf16 v[74:77], v[26:29], v[192:199], v1\n" \
    "s_waitcnt lgkmcnt(8)\n" \
    "v_smfmac_f32_16x16x64_bf16 v[82:85], v[26:29], v[168:175], v1\n" \
    "ds_read_b128 v[168:171], v17 offset:24576\n" \
    "ds_read_b128 v[172:175], v17 offset:25600\n" \
    "v_smfmac_f32_16x16x64_bf16 v[74:77], v[30:33], v[200:207], v1\n" \
    "s_waitcnt lgkmcnt(8)\n" \
    "v_smfmac_f32_16x16x64_bf16 v[82:85], v[30:33], v[176:183], v1\n" \
    "ds_read_b128 v[176:179], v17 offset:26624\n" \
    "ds_read_b128 v[180:183], v17 offset:27648\n" \
    "v_smfmac_f32_16x16x64_bf16 v[74:77], v[34:37], v[208:215], v1\n" \
    "s_waitcnt lgkmcnt(8)\n" \
    "v_smfmac_f32_16x16x64_bf16 v[82:85], v[34:37], v[184:191], v1\n" \
    "v_add_f32 v70, v70, v71\n" \
    "v_add_f32 v72, v72, v73\n" \
    "v_cvt_pk_bf16_f32 v144, v70, v72\n" \
    "v_smfmac_f32_16x16x64_bf16 v[74:77], v[38:41], v[216:223], v1\n" \
    "s_waitcnt lgkmcnt(6)\n" \
    "v_smfmac_f32_16x16x64_bf16 v[82:85], v[38:41], v[152:159], v1\n" \
    "v_add_f32 v78, v78, v79\n" \
    "v_add_f32 v80, v80, v81\n" \
    "v_cvt_pk_bf16_f32 v146, v78, v80\n" \
    "v_smfmac_f32_16x16x64_bf16 v[74:77], v[42:45], v[224:231], v1\n" \
    "s_waitcnt lgkmcnt(4)\n" \
    "v_smfmac_f32_16x16x64_bf16 v[82:85], v[42:45], v[160:167], v1\n" \
    "v_smfmac_f32_16x16x64_bf16 v[74:77], v[46:49], v[232:239], v1\n" \
    "s_waitcnt lgkmcnt(2)\n" \
    "v_smfmac_f32_16x16x64_bf16 v[82:85], v[46:49], v[168:175], v1\n" \
    "buffer_store_dwordx4 v[118:121], v19, s[48:51], s64 offen sc1\n" \
    "v_smfmac_f32_16x16x64_bf16 v[74:77], v[50:53], v[240:247], v1\n" \
    "s_waitcnt lgkmcnt(0)\n" \
    "v_smfmac_f32_16x16x64_bf16 v[82:85], v[50:53], v[176:183], v1\n" \
    "s_nop 5\n" \
    "v_add_f32 v74, v74, v75\n" \
    "v_add_f32 v76, v76, v77\n" \
    "v_cvt_pk_bf16_f32 v145, v74, v76\n" \
    "v_add_f32 v82, v82, v83\n" \
    "v_add_f32 v84, v84, v85\n" \
    "v_cvt_pk_bf16_f32 v147, v82, v84\n" \
    "v_permlane32_swap_b32 v144, v146\n" \
    "s_nop 0\n" \
    "v_permlane32_swap_b32 v145, v147\n" \
    "s_nop 1\n" \
    "v_permlane16_swap_b32 v144, v145\n" \
    "v_permlane16_swap_b32 v146, v147\n" \
    "s_cmp_eq_u32 s69, 0\n" \
    "s_cbranch_scc1 LnoVMW3_%=\n" \
    "buffer_store_dwordx4 v[144:147], v0, s[52:55], s65 offen sc1\n" \
    "LnoVMW3_%=:\n" \
    "s_memtime s[78:79]\n" \
    "s_waitcnt lgkmcnt(0)\n" \
    "s_sub_u32 s67, s78, s76\n" \
    "s_sub_i32 s67, 790, s67\n" \
    "s_cmp_lt_i32 s67, 36\n" \
    "s_cbranch_scc1 Ltg0W3_%=\n" \
    "s_mul_i32 s67, s67, 29\n" \
    "s_lshr_b32 s67, s67, 10\n" \
    "Ltl0W3_%=:\n" \
    "s_sub_u32 s67, s67, 1\n" \
    "s_cmp_lg_u32 s67, 0\n" \
    "s_cbranch_scc1 Ltl0W3_%=\n" \
    "Ltg0W3_%=:\n" \
    "buffer_load_dwordx4 v[134:137], v13, s[36:39], s61 offen sc1\n" \
    "s_nop 15\n" \
    "s_nop 15\n" \
    "buffer_load_dwordx4 v[102:105], v11, s[36:39], s61 offen sc1\n" \
    "buffer_load_dwordx4 v[110:113], v11, s[36:39], s61 offen offset:1024 sc1\n" \
    "buffer_load_dwordx2 v[142:143], v12, s[36:39], s61 offen sc1\n" \
    "s_nop 15\n" \
    "s_nop 15\n" \
    "s_nop 14\n" \
    "buffer_load_dwordx4 v[134:137], v13, s[36:39], s61 offen sc1\n" \
    "s_cmp_eq_u32 s60, 63\n" \
    "s_cbranch_scc1 LskipW3_%=\n" \
    "s_waitcnt vmcnt(4)\n" \
    "v_max3_u32 v248, v134, v135, v136\n" \
    "v_max_u32 v248, v248, v137\n" \
    "v_cmp_eq_u32 vcc, -1, v248\n" \
    "s_cbranch_vccz LokPW3_%=\n" \
    "s_add_u32 s84, s84, 0x100000\n" \
    "s_waitcnt vmcnt(0)\n" \
    "v_max3_u32 v248, v134, v135, v136\n" \
    "v_max_u32 v248, v248, v137\n" \
    "v_cmp_eq_u32 vcc, -1, v248\n" \
    "s_cbranch_vccz LokPW3_%=\n" \
    "s_add_u32 s84, s84, 0x100000\n" \
    "s_branch LslowPW3_%=\n" \
    "LokPW3_%=:\n" \
    "s_nop 0\n" \
    "v_cndmask_b32 v62, v136, v134, s[58:59]\n" \
    "v_cndmask_b32 v63, v137, v135, s[58:59]\n" \
    "v_cndmask_b32 v64, v134, v136, s[58:59]\n" \
    "v_cndmask_b32 v65, v135, v137, s[58:59]\n" \
    "v_mov_b32_dpp v62, v62 row_ror:8 row_mask:0xf bank_mask:0xf bound_ctrl:1\n" \
    "v_mov_b32_dpp v63, v63 row_ror:8 row_mask:0xf bank_mask:0xf bound_ctrl:1\n" \
    "v_lshlrev_b32 v54, 16, v64\n" \
    "v_and_b32 v55, 0xffff0000, v64\n" \
    "v_lshlrev_b32 v56, 16, v65\n" \
    "v_and_b32 v57, 0xffff0000, v65\n" \
    "v_lshlrev_b32 v58, 16, v62\n" \
    "v_and_b32 v59, 0xffff0000, v62\n" \
    "v_lshlrev_b32 v60, 16, v63\n" \
    "v_and_b32 v61, 0xffff0000, v63\n" \
    "v_pk_add_f32 v[54:55], v[54:55], v[58:59]\n" \
    "v_pk_add_f32 v[56:57], v[56:57], v[60:61]\n" \
    "v_max_f32 v66, v54, v55\n" \
    "v_max3_f32 v66, v66, v56, v57\n" \
    "s_nop 1\n" \
    "v_max_f32_dpp v66, v66, v66 row_ror:8 row_mask:0xf bank_mask:0xf\n" \
    "v_pk_add_f32 v[62:63], v[54:55], v[66:67] op_sel_hi:[1,0] neg_lo:[0,1] neg_hi:[0,1]\n" \
    "v_pk_add_f32 v[64:65], v[56:57], v[66:67] op_sel_hi:[1,0] neg_lo:[0,1] neg_hi:[0,1]\n" \
    "v_exp_f32 v62, v62\n" \
    "v_exp_f32 v63, v63\n" \
    "v_exp_f32 v64, v64\n" \
    "v_exp_f32 v65, v65\n" \
    "s_nop 0\n" \
    "v_pk_add_f32 v[248:249], v[62:63], v[64:65]\n" \
    "v_add_f32 v68, v248, v249\n" \
    "s_nop 1\n" \
    "v_add_f32_dpp v68, v68, v68 row_ror:8 row_mask:0xf bank_mask:0xf\n" \
    "s_waitcnt vmcnt(1)\n" \
    "v_max3_u32 v248, v102, v103, v104\n" \
    "v_max3_u32 v249, v105, v110, v111\n" \
    "v_max3_u32 v248, v248, v112, v113\n" \
    "v_max3_u32 v249, v249, v142, v143\n" \
    "v_max_u32 v248, v248, v249\n" \
    "v_cmp_eq_u32 vcc, -1, v248\n" \
    "s_cbranch_vccz LokEW3_%=\n" \
    "s_add_u32 s84, s84, 0x400\n" \
    "s_branch LslowEW3_%=\n" \
    "LokEW3_%=:\n" \
    "s_nop 0\n" \
    "v_mov_b32_dpp v106, v102 row_ror:8 row_mask:0xf bank_mask:0xf bound_ctrl:1\n" \
    "v_mov_b32_dpp v107, v103 row_ror:8 row_mask:0xf bank_mask:0xf bound_ctrl:1\n" \
    "v_mov_b32_dpp v108, v104 row_ror:8 row_mask:0xf bank_mask:0xf bound_ctrl:1\n" \
    "v_mov_b32_dpp v109, v105 row_ror:8 row_mask:0xf bank_mask:0xf bound_ctrl:1\n" \
    "v_mfma_f32_16x16x32_bf16 v[118:121], v[86:89], v[102:105], 0\n" \
    "v_max_f32 v58, v66, v142\n" \
    "v_mfma_f32_16x16x32_bf16 v[126:129], v[94:97], v[110:113], 0\n" \
    "v_sub_f32 v59, v66, v58\n" \
    "v_sub_f32 v60, v142, v58\n" \
    "v_mfma_f32_16x16x32_bf16 v[122:125], v[90:93], v[106:109], 0\n" \
    "v_exp_f32 v59, v59\n" \
    "v_exp_f32 v60, v60\n" \
    "v_mov_b32_dpp v114, v110 row_ror:8 row_mask:0xf bank_mask:0xf bound_ctrl:1\n" \
    "v_mov_b32_dpp v115, v111 row_ror:8 row_mask:0xf bank_mask:0xf bound_ctrl:1\n" \
    "v_mov_b32_dpp v116, v112 row_ror:8 row_mask:0xf bank_mask:0xf bound_ctrl:1\n" \
    "v_mov_b32_dpp v117, v113 row_ror:8 row_mask:0xf bank_mask:0xf bound_ctrl:1\n" \
    "v_mul_f32 v61, v59, v68\n" \
    "s_nop 0\n" \
    "v_mfma_f32_16x16x32_bf16 v[130:133], v[98:101], v[114:117], 0\n" \
    "v_fmac_f32 v61, v60, v143\n" \
    "v_rcp_f32 v61, v61\n" \
    "s_nop 0\n" \
    "v_mul_f32 v66, v59, v61\n" \
    "v_mul_f32 v250, v60, v61\n" \
    "v_pk_mul_f32 v[62:63], v[62:63], v[66:67] op_sel_hi:[1,0]\n" \
    "v_pk_mul_f32 v[64:65], v[64:65], v[66:67] op_sel_hi:[1,0]\n" \
    "v_cvt_pk_bf16_f32 v148, v62, v63\n" \
    "v_cvt_pk_bf16_f32 v149, v64, v65\n" \
    "v_mov_b32 v54, v250\n" \
    "v_mov_b32_dpp v150, v148 row_ror:8 row_mask:0xf bank_mask:0xf bound_ctrl:1\n" \
    "v_mov_b32_dpp v151, v149 row_ror:8 row_mask:0xf bank_mask:0xf bound_ctrl:1\n" \
    "v_permlane16_swap_b32 v250, v54\n" \
    "s_nop 0\n" \
    "v_mfma_f32_16x16x32_bf16 v[62:65], v[144:147], v[148:151], 0\n" \
    "v_mov_b32 v58, v250\n" \
    "v_mov_b32 v56, v54\n" \
    "s_nop 0\n" \
    "v_permlane32_swap_b32 v250, v58\n" \
    "v_permlane32_swap_b32 v54, v56\n" \
    "v_pk_mul_f32 v[138:139], v[118:119], v[250:251] op_sel_hi:[1,0]\n" \
    "v_pk_mul_f32 v[140:141], v[120:121], v[250:251] op_sel_hi:[1,0]\n" \
    "v_pk_fma_f32 v[138:139], v[126:127], v[58:59], v[138:139] op_sel_hi:[1,0,1]\n" \
    "v_pk_fma_f32 v[140:141], v[128:129], v[58:59], v[140:141] op_sel_hi:[1,0,1]\n" \
    "v_pk_fma_f32 v[138:139], v[122:123], v[54:55], v[138:139] op_sel_hi:[1,0,1]\n" \
    "v_pk_fma_f32 v[140:141], v[124:125], v[54:55], v[140:141] op_sel_hi:[1,0,1]\n" \
    "v_pk_fma_f32 v[138:139], v[130:131], v[56:57], v[138:139] op_sel_hi:[1,0,1]\n" \
    "v_pk_fma_f32 v[140:141], v[132:133], v[56:57], v[140:141] op_sel_hi:[1,0,1]\n" \
    "v_pk_add_f32 v[138:139], v[138:139], v[62:63]\n" \
    "v_pk_add_f32 v[140:141], v[140:141], v[64:65]\n" \
    "s_mov_b64 exec, s[74:75]\n" \
    "ds_write_b128 v14, v[138:141]\n" \
    "s_mov_b64 exec, -1\n" \
    "LskipW3_%=:\n" \
    "s_waitcnt lgkmcnt(0)\n" \
    "s_barrier\n" \
    "s_cmp_eq_u32 s60, 63\n" \
    "s_cbranch_scc1 LnoPubW3_%=\n" \
    "s_cmp_eq_u32 s69, 0\n" \
    "s_cbranch_scc1 LnoPubW3_%=\n" \
    "ds_read_b64 v[152:153], v15\n" \
    "ds_read_b64 v[154:155], v15 offset:512\n" \
    "ds_read_b64 v[156:157], v15 offset:1024\n" \
    "ds_read_b64 v[158:159], v15 offset:1536\n" \
    "s_waitcnt lgkmcnt(2)\n" \
    "v_add_f32 v152, v152, v154\n" \
    "v_add_f32 v153, v153, v155\n" \
    "s_waitcnt lgkmcnt(0)\n" \
    "v_add_f32 v156, v156, v158\n" \
    "v_add_f32 v157, v157, v159\n" \
    "v_add_f32 v152, v152, v156\n" \
    "v_add_f32 v153, v153, v157\n" \
    "v_cvt_pk_bf16_f32 v152, v152, v153\n" \
    "v_min_u32 v152, -2, v152\n" \
    "buffer_store_dword v152, v16, s[36:39], s62 offen sc1\n" \
    "s_mov_b64 exec, 1\n" \
    "buffer_store_dword v0, v24, s[36:39], s62 offen sc1\n" \
    "s_mov_b64 exec, -1\n" \
    "LnoPubW3_%=:\n" \
    "s_add_u32 s60, s60, 1\n" \
    "s_mov_b32 s61, s62\n" \
    "s_add_u32 s67, s60, 1\n" \
    "s_lshr_b32 s68, s67, 5\n" \
    "s_lshl_b32 s68, s68, 4\n" \
    "s_lshr_b32 s68, s72, s68\n" \
    "s_and_b32 s68, s68, 0x2000\n" \
    "s_lshl_b32 s67, s67, 16\n" \
    "s_add_u32 s62, s67, s68\n" \
    "s_add_u32 s63, s63, 458752\n" \
    "s_add_u32 s64, s64, 131072\n" \
    "s_add_u32 s65, s65, 114688\n" \
    "s_cmp_ge_u32 s60, s70\n" \
    "s_cbranch_scc1 LendW3_%=\n" \
    "v_readfirstlane_b32 s67, v254\n" \
    "s_nop 1\n" \
    "s_cmp_lt_u32 s67, 160\n" \
    "s_cbranch_scc1 LslowTW3_%=\n" \
    "LbackTW3_%=:\n" \
    "s_mov_b32 s67, s66\n" \
    "s_cmp_eq_u32 s67, 0\n" \
    "s_cbranch_scc1 LdAW3_%=_e\n" \
    "LdAW3_%=:\n" \
    "s_sub_u32 s67, s67, 1\n" \
    "s_cmp_lg_u32 s67, 0\n" \
    "s_cbranch_scc1 LdAW3_%=\n" \
    "LdAW3_%=_e:\n" \
    "s_branch LloopW3_%=\n" \
    "LslowMW3_%=:\n" \
    "s_mov_b32 s87, 0\n" \
    "buffer_load_dwordx4 v[26:29], v3, s[36:39], s61 offen sc1\n" \
    "buffer_load_dwordx4 v[30:33], v4, s[36:39], s61 offen sc1\n" \
    "s_nop 15\n" \
    "s_nop 15\n" \
    "s_nop 7\n" \
    "LslowMlW3_%=:\n" \
    "s_add_u32 s84, s84, 1\n" \
    "buffer_load_dwordx4 v[26:29], v3, s[36:39], s61 offen sc1\n" \
    "buffer_load_dwordx4 v[30:33], v4, s[36:39], s61 offen sc1\n" \
    "s_waitcnt vmcnt(2)\n" \
    "v_max3_u32 v248, v26, v27, v28\n" \
    "v_max3_u32 v249, v29, v30, v31\n" \
    "v_max3_u32 v248, v248, v32, v33\n" \
    "v_max_u32 v248, v248, v249\n" \
    "v_cmp_eq_u32 vcc, -1, v248\n" \
    "s_cbranch_vccz LokMW3_%=\n" \
    "s_add_u32 s87, s87, 1\n" \
    "s_cmp_lt_u32 s87, 0x8000\n" \
    "s_cbranch_scc1 LslowMlW3_%=\n" \
    "s_branch LokMW3_%=\n" \
    "LslowEW3_%=:\n" \
    "s_mov_b32 s87, 0\n" \
    "buffer_load_dwordx4 v[102:105], v11, s[36:39], s61 offen sc1\n" \
    "buffer_load_dwordx4 v[110:113], v11, s[36:39], s61 offen offset:1024 sc1\n" \
    "buffer_load_dwordx2 v[142:143], v12, s[36:39], s61 offen sc1\n" \
    "s_nop 15\n" \
    "s_nop 15\n" \
    "s_nop 7\n" \
    "LslowElW3_%=:\n" \
    "s_add_u32 s84, s84, 1024\n" \
    "buffer_load_dwordx4 v[102:105], v11, s[36:39], s61 offen sc1\n" \
    "buffer_load_dwordx4 v[110:113], v11, s[36:39], s61 offen offset:1024 sc1\n" \
    "buffer_load_dwordx2 v[142:143], v12, s[36:39], s61 offen sc1\n" \
    "s_waitcnt vmcnt(3)\n" \
    "v_max3_u32 v248, v102, v103, v104\n" \
    "v_max3_u32 v249, v105, v110, v111\n" \
    "v_max3_u32 v248, v248, v112, v113\n" \
    "v_max3_u32 v249, v249, v142, v143\n" \
    "v_max_u32 v248, v248, v249\n" \
    "v_cmp_eq_u32 vcc, -1, v248\n" \
    "s_cbranch_vccz LokEW3_%=\n" \
    "s_add_u32 s87, s87, 1\n" \
    "s_cmp_lt_u32 s87, 0x8000\n" \
    "s_cbranch_scc1 LslowElW3_%=\n" \
    "s_branch LokEW3_%=\n" \
    "LslowPW3_%=:\n" \
    "s_mov_b32 s87, 0\n" \
    "buffer_load_dwordx4 v[134:137], v13, s[36:39], s61 offen sc1\n" \
    "s_nop 15\n" \
    "s_nop 15\n" \
    "s_nop 7\n" \
    "LslowPlW3_%=:\n" \
    "s_add_u32 s84, s84, 1048576\n" \
    "buffer_load_dwordx4 v[134:137], v13, s[36:39], s61 offen sc1\n" \
    "s_waitcnt vmcnt(1)\n" \
    "v_max3_u32 v248, v134, v135, v136\n" \
    "v_max_u32 v248, v248, v137\n" \
    "v_cmp_eq_u32 vcc, -1, v248\n" \
    "s_cbranch_vccz LokPW3_%=\n" \
    "s_add_u32 s87, s87, 1\n" \
    "s_cmp_lt_u32 s87, 0x8000\n" \
    "s_cbranch_scc1 LslowPlW3_%=\n" \
    "s_branch LokPW3_%=\n" \
    "LslowTW3_%=:\n" \
    "s_mov_b32 s87, 0\n" \
    "s_lshr_b32 s68, s60, 2\n" \
    "s_lshl_b32 s68, s68, 8\n" \
    "v_mov_b32 v253, s68\n" \
    "LslowTlW3_%=:\n" \
    "global_load_dword v254, v253, s[40:41] sc1\n" \
    "s_waitcnt vmcnt(0)\n" \
    "v_readfirstlane_b32 s67, v254\n" \
    "s_nop 1\n" \
    "s_cmp_ge_u32 s67, 160\n" \
    "s_cbranch_scc1 LbackTW3_%=\n" \
    "s_sleep 2\n" \
    "s_add_u32 s87, s87, 1\n" \
    "s_cmp_lt_u32 s87, 0x40000\n" \
    "s_cbranch_scc1 LslowTlW3_%=\n" \
    "s_branch LbackTW3_%=\n" \
    "LendW3_%=:\n" \
    "s_waitcnt vmcnt(0)\n" \
    "s_barrier\n" \
    "s_lshl_b32 s67, s70, 8\n" \
    "s_sub_u32 s67, s67, 256\n" \
    "v_mov_b32 v248, s67\n" \
    "v_mov_b32 v249, 1\n" \
    "s_mov_b64 exec, 1\n" \
    "global_atomic_add v248, v249, s[56:57] sc1\n" \
    "s_mov_b64 exec, -1\n" \
    "s_branch Ldone_%=\n" \
    "Ldone_%=:\n" \
    ""
#define CHAIN_ASM_CLOBBERS "v0", "v1", "v2", "v3", "v4", "v5", "v6", "v7", "v8", "v9", "v10", "v11", "v12", "v13", "v14", "v15", "v16", "v17", "v18", "v19", "v20", "v21", "v22", "v23", "v24", "v25", "v26", "v27", "v28", "v29", "v30", "v31", "v32", "v33", "v34", "v35", "v36", "v37", "v38", "v39", "v40", "v41", "v42", "v43", "v44", "v45", "v46", "v47", "v48", "v49", "v50", "v51", "v52", "v53", "v54", "v55", "v56", "v57", "v58", "v59", "v60", "v61", "v62", "v63", "v64", "v65", "v66", "v67", "v68", "v69", "v70", "v71", "v72", "v73", "v74", "v75", "v76", "v77", "v78", "v79", "v80", "v81", "v82", "v83", "v84", "v85", "v86", "v87", "v88", "v89", "v90", "v91", "v92", "v93", "v94", "v95", "v96", "v97", "v98", "v99", "v100", "v101", "v102", "v103", "v104", "v105", "v106", "v107", "v108", "v109", "v110", "v111", "v112", "v113", "v114", "v115", "v116", "v117", "v118", "v119", "v120", "v121", "v122", "v123", "v124", "v125", "v126", "v127", "v128", "v129", "v130", "v131", "v132", "v133", "v134", "v135", "v136", "v137", "v138", "v139", "v140", "v141", "v142", "v143", "v144", "v145", "v146", "v147", "v148", "v149", "v150", "v151", "v152", "v153", "v154", "v155", "v156", "v157", "v158", "v159", "v160", "v161", "v162", "v163", "v164", "v165", "v166", "v167", "v168", "v169", "v170", "v171", "v172", "v173", "v174", "v175", "v176", "v177", "v178", "v179", "v180", "v181", "v182", "v183", "v184", "v185", "v186", "v187", "v188", "v189", "v190", "v191", "v192", "v193", "v194", "v195", "v196", "v197", "v198", "v199", "v200", "v201", "v202", "v203", "v204", "v205", "v206", "v207", "v208", "v209", "v210", "v211", "v212", "v213", "v214", "v215", "v216", "v217", "v218", "v219", "v220", "v221", "v222", "v223", "v224", "v225", "v226", "v227", "v228", "v229", "v230", "v231", "v232", "v233", "v234", "v235", "v236", "v237", "v238", "v239", "v240", "v241", "v242", "v243", "v244", "v245", "v246", "v247", "v248", "v249", "v250", "v251", "v252", "v253", "v254", "v255", "a0", "a1", "a2", "a3", "a4", "a5", "a6", "a7", "a8", "a9", "a10", "a11", "a12", "a13", "a14", "a15", "a16", "a17", "a18", "a19", "a20", "a21", "a22", "a23", "a24", "a25", "a26", "a27", "a28", "a29", "a30", "a31", "a32", "a33", "a34", "a35", "a36", "a37", "a38", "a39", "a40", "a41", "a42", "a43", "a44", "a45", "a46", "a47", "a48", "a49", "a50", "a51", "a52", "a53", "a54", "a55", "a56", "a57", "a58", "a59", "a60", "a61", "a62", "a63", "a64", "a65", "a66", "a67", "a68", "a69", "a70", "a71", "a72", "a73", "a74", "a75", "a76", "a77", "a78", "a79", "a80", "a81", "a82", "a83", "a84", "a85", "a86", "a87", "a88", "a89", "a90", "a91", "a92", "a93", "a94", "a95", "a96", "a97", "a98", "a99", "a100", "a101", "a102", "a103", "a104", "a105", "a106", "a107", "a108", "a109", "a110", "a111", "a112", "a113", "a114", "a115", "a116", "a117", "a118", "a119", "a120", "a121", "a122", "a123", "a124", "a125", "a126", "a127", "a128", "a129", "a130", "a131", "a132", "a133", "a134", "a135", "a136", "a137", "a138", "a139", "a140", "a141", "a142", "a143", "a144", "a145", "a146", "a147", "a148", "a149", "a150", "a151", "a152", "a153", "a154", "a155", "a156", "a157", "a158", "a159", "a160", "a161", "a162", "a163", "a164", "a165", "a166", "a167", "a168", "a169", "a170", "a171", "a172", "a173", "a174", "a175", "a176", "a177", "a178", "a179", "a180", "a181", "a182", "a183", "a184", "a185", "a186", "a187", "a188", "a189", "a190", "a191", "a192", "a193", "a194", "a195", "a196", "a197", "a198", "a199", "a200", "a201", "a202", "a203", "a204", "a205", "a206", "a207", "a208", "a209", "a210", "a211", "a212", "a213", "a214", "a215", "a216", "a217", "a218", "a219", "a220", "a221", "a222", "a223", "a224", "a225", "a226", "a227", "a228", "a229", "a230", "a231", "a232", "a233", "a234", "a235", "a236", "a237", "a238", "a239", "a240", "a241", "a242", "a243", "a244", "a245", "a246", "a247", "a248", "a249", "a250", "a251", "a252", "a253", "a254", "a255", "s36", "s37", "s38", "s39", "s40", "s41", "s42", "s43", "s44", "s45", "s46", "s47", "s48", "s49", "s50", "s51", "s52", "s53", "s54", "s55", "s56", "s57", "s58", "s59", "s60", "s61", "s62", "s63", "s64", "s65", "s66", "s67", "s68", "s69", "s70", "s71", "s72", "s73", "s74", "s75", "s76", "s77", "s78", "s79", "s80", "s81", "s82", "s83", "s84", "s85", "s86", "s87", "s88", "s89", "s90", "s91", "s92", "s93", "s94", "s95", "s96", "s97", "s98", "s99", "vcc", "scc", "memory"
#define CH_L_CONST 0
#define CH_NCONST 25
#define CH_L_END 128000
#define CH_L_WB 0
#define CH_L_MEM 114688
#define CH_L_UT 121856
#define CH_L_P 125952
#define CH_NLW 14
#define CH_DA 16
#define CH_DB 0

struct ChainBlock {
    unsigned rH[4], rVT[4], rUM[4], rVM[4], rWA[4], rWB[4];
    unsigned long long cnt, til, dbg, pad0;
    unsigned s, hb, hbn, vto, umo, vmo, dA, tmp, tmp2, col, steps, dB, par, pad1;
    unsigned long long mN8, mLO32;
    unsigned fail, wao, wbo, pad2;
    unsigned pad3[10];
};
static_assert(sizeof(ChainBlock) == 256, "layout");

struct Params {
    const u16 *x, *wqkv, *wphi, *wo, *mem0, *Wq, *Wk, *Wv, *Wo, *gw;
    u16* out;
    char* ws;
};

#define TAIL_SLEEP 2
typedef __attribute__((address_space(4))) const Params KParams;
DI int opaque_tid() { int t = threadIdx.x; asm volatile("" : "+v"(t)); return t; }
DI KParams* kparams() { KParams* k = (KParams*)__builtin_amdgcn_kernarg_segment_ptr(); asm volatile("" : "+s"(k)); return k; }
DI float bf2f(u16 v) { return __uint_as_float((unsigned)v << 16); }
DI float rbf(float f) { return (float)(__bf16)f; }
DI unsigned cvtpk(float a, float b) { bf16x2 v = {(__bf16)a, (__bf16)b}; return __builtin_bit_cast(unsigned, v); }
DI float blo(unsigned v) { return __uint_as_float(v << 16); }
DI float bhi(unsigned v) { return __uint_as_float(v & 0xffff0000u); }
DI unsigned nosent(unsigned v) { return min(v, 0xFFFFFFFEu); }
DI unsigned mx4(u32x4 v) { return max(max(v.x, v.y), max(v.z, v.w)); }
DI int pi32(int m) { return (m & 0x13) | ((m & 4) << 1) | ((m & 8) >> 1); }
template <int CTRL> DI float dppf(float v) { return __builtin_bit_cast(float, __builtin_amdgcn_update_dpp(0, __builtin_bit_cast(int, v), CTRL, 0xf, 0xf, true)); }
DI float xhalf_max(float v) { auto r = __builtin_amdgcn_permlane32_swap(__float_as_uint(v), __float_as_uint(v), false, false); return fmaxf(__uint_as_float(r[0]), __uint_as_float(r[1])); }
DI float xhalf_sum(float v) { auto r = __builtin_amdgcn_permlane32_swap(__float_as_uint(v), __float_as_uint(v), false, false); return __uint_as_float(r[0]) + __uint_as_float(r[1]); }
DI bf16x16 mk16(u32x4 a, u32x4 b) { u32x8 r = {a.x, a.y, a.z, a.w, b.x, b.y, b.z, b.w}; return __builtin_bit_cast(bf16x16, r); }
DI bf16x8 asfrag(u32x4 v) { return __builtin_bit_cast(bf16x8, v); }
DI bf16x8 pack8(const f32x16& a, int s, float sc) {
    u32x4 p = {cvtpk(a[8 * s] * sc, a[8 * s + 1] * sc), cvtpk(a[8 * s + 2] * sc, a[8 * s + 3] * sc), cvtpk(a[8 * s + 4] * sc, a[8 * s + 5] * sc), cvtpk(a[8 * s + 6] * sc, a[8 * s + 7] * sc)};
    return asfrag(p);
}
#define MFMA32(a, b, c) __builtin_amdgcn_mfma_f32_32x32x16_bf16((a), (b), (c), 0, 0, 0)
#define SMF(a, b, c, idx) __builtin_amdgcn_smfmac_f32_16x16x64_bf16((a), (b), (c), (idx), 0, 0)
#define SCHED() __builtin_amdgcn_sched_barrier(0)
#define BARRIER() do { asm volatile("s_waitcnt lgkmcnt(0)" ::: "memory"); __builtin_amdgcn_s_barrier(); asm volatile("" ::: "memory"); } while (0)
typedef __amdgpu_buffer_rsrc_t rsrc_t;
DI rsrc_t mkrs(const void* p, unsigned bytes) { return __builtin_amdgcn_make_buffer_rsrc((void*)p, 0, bytes, 0x00020000); }
#define AUX_SC1 16
DI u32x4 ld16c(rsrc_t r, unsigned off) { return __builtin_amdgcn_raw_buffer_load_b128(r, off, 0, AUX_SC1); }
DI u32x2 ld8c(rsrc_t r, unsigned off) { return __builtin_amdgcn_raw_buffer_load_b64(r, off, 0, AUX_SC1); }
DI void st16c(rsrc_t r, unsigned off, u32x4 v) { __builtin_amdgcn_raw_buffer_store_b128(v, r, off, 0, AUX_SC1); }
DI void st8c(rsrc_t r, unsigned off, u32x2 v) { __builtin_amdgcn_raw_buffer_store_b64(v, r, off, 0, AUX_SC1); }
DI void st4c(rsrc_t r, unsigned off, unsigned v) { __builtin_amdgcn_raw_buffer_store_b32(v, r, off, 0, AUX_SC1); }
DI GAS unsigned* cntp(char* ws, int i) { return (GAS unsigned*)(ws + WS_CNT + (size_t)i * 256); }
DI void signal_done(char* ws, int i, unsigned add = 1u, int i2 = -1) {
    asm volatile("s_waitcnt vmcnt(0)" ::: "memory");
    __builtin_amdgcn_s_barrier();
    if (threadIdx.x == 0) {
        if (add) __hip_atomic_fetch_add(cntp(ws, i), add, __ATOMIC_RELAXED, __HIP_MEMORY_SCOPE_AGENT);
        if (i2 >= 0) __hip_atomic_fetch_add(cntp(ws, i2), 1u, __ATOMIC_RELAXED, __HIP_MEMORY_SCOPE_AGENT);
    }
}
DI void wait_count(char* ws, int i, unsigned target, int nap = 8) {
    if (threadIdx.x < 64) {
#pragma clang loop unroll(disable)
        for (unsigned spins = 0; spins < (1u << 20); ++spins) {
            const unsigned v = __hip_atomic_load(cntp(ws, i), __ATOMIC_RELAXED, __HIP_MEMORY_SCOPE_AGENT);
            if (__builtin_amdgcn_readfirstlane(v) >= target) break;
            if (nap == 8) __builtin_amdgcn_s_sleep(8); else __builtin_amdgcn_s_sleep(2);
        }
    }
    BARRIER();
}

#define TL(slot, ev)
#define TLF(ev)
constexpr int PL_Q = 28672 + 16384, PL_END = 3 * PL_Q;

template <int V> struct IC { static constexpr int value = V; };
template <int B, int E, class F> DI void static_for(F&& f) { if constexpr (B < E) { f(IC<B>{}); static_for<B + 1, E>(f); } }
DI void glds16v(const void* gsrc, unsigned lds_dst) {
    unsigned keep;
    asm volatile("s_mov_b32 %0, m0\n\ts_mov_b32 m0, %2\n\ts_nop 0\n\tglobal_load_lds_dwordx4 %1, off\n\ts_mov_b32 m0, %0" : "=&s"(keep) : "v"(gsrc), "s"(lds_dst) : "memory");
}

__global__ void __launch_bounds__(256, 1) prep_kernel(Params p) {
    extern __shared__ __attribute__((aligned(16))) char lds[];
    const int b = blockIdx.x, tid = threadIdx.x, lane = tid & 63, w = __builtin_amdgcn_readfirstlane(tid >> 6);
    char* ws = p.ws;
    TL(256 + b, 0);
    {
        const int mat = (b & 7) + 8 * ((b >> 3) & 3), sub = b >> 5, type = mat >> 4, h = mat & 15, ch = sub & 1, og = sub >> 1, ot = min(4 * og + w, 13);
        const bool act = 4 * og + w < 14;
        const int m = lane & 31, kh = lane >> 5;
        const int pr = lane >> 3, pc = (lane & 7) ^ pr;
        const u16* cs = (type == 0 ? p.Wk : p.Wv) + (size_t)(224 * ch + 8 * (7 * w) + pr) * 4096 + h * 256 + 8 * pc;
        const u16* os = type == 0 ? p.Wq + (size_t)(32 * ot + pr) * 4096 + h * 256 + 8 * pc : p.Wo + (size_t)(h * 256 + (lane >> 2)) * D + 32 * ot + 8 * (lane & 3);
        auto issue = [&](int q) {
            const unsigned base = (unsigned)((q % 3) * PL_Q);
#pragma unroll
            for (int i = 0; i < 7; ++i) glds16v(cs + (size_t)8 * i * 4096 + 64 * q, base + (7 * w + i) * 1024);
#pragma unroll
            for (int i = 0; i < 4; ++i) glds16v(type == 0 ? os + (size_t)8 * i * 4096 + 64 * q : os + (size_t)(64 * q + 16 * i) * D, base + 28672 + w * 4096 + i * 1024);
        };
        issue(0); issue(1); issue(2);
        f32x16 acc[7];
#pragma unroll
        for (int t = 0; t < 7; ++t)
#pragma unroll
            for (int i = 0; i < 16; ++i) acc[t][i] = 0.f;
        static_for<0, 4>([&](auto Q) {
            constexpr int q = Q.value;
            if constexpr (q == 0) asm volatile("s_waitcnt vmcnt(22)" ::: "memory");
            else if constexpr (q == 1) asm volatile("s_waitcnt vmcnt(11)" ::: "memory");
            else if constexpr (q == 2) asm volatile("s_waitcnt vmcnt(11)" ::: "memory");
            else asm volatile("s_waitcnt vmcnt(0)" ::: "memory");
            BARRIER();
            TL(256 + b, 4 + q);
            if constexpr (q == 1) issue(3);
            const char* cb = lds + (q % 3) * PL_Q;
            const char* ob = cb + 28672 + w * 4096;
            bf16x8 bf[4];
#pragma unroll
            for (int k = 0; k < 4; ++k) {
                if (type == 0) { const int r = pi32(m); bf[k] = *(LAS bf16x8*)(ob + r * 128 + (((2 * k + kh) ^ (r & 7)) << 4)); }
                else {
                    const int blk = (lane >> 4) & 1, qq = (lane >> 2) & 3, pp = lane & 3;
                    const char* a = ob + (16 * k + 8 * kh + qq) * 64 + blk * 32 + pp * 8;
                    const bf16x4 lo = __builtin_amdgcn_ds_read_tr16_b64_v4bf16((LAS bf16x4*)a), hi = __builtin_amdgcn_ds_read_tr16_b64_v4bf16((LAS bf16x4*)(a + 256));
                    bf[k] = __builtin_shufflevector(lo, hi, 0, 1, 2, 3, 4, 5, 6, 7);
                }
            }
            const int rr = pi32(m);
            const char* ab = cb + rr * 128;
            auto rda = [&](int f) { return *(LAS bf16x8*)(ab + (f % 7) * 4096 + (((2 * (f / 7) + kh) ^ (rr & 7)) << 4)); };
            bf16x8 af[4];
#pragma unroll
            for (int f = 0; f < 3; ++f) af[f] = rda(f);
            static_for<0, 28>([&](auto F) {
                constexpr int f = F.value;
                if constexpr (f + 3 < 28) af[(f + 3) & 3] = rda(f + 3);
                acc[f % 7] = MFMA32(af[f & 3], bf[f / 7], acc[f % 7]);
                SCHED();
            });
        });
        if (act) {
            char* dst = ws + (type == 0 ? WS_WAF : WS_WBF) + (size_t)h * 28 * FR + ot * 1024 + lane * 16;
            const float sc = type == 0 ? LOG2E / 16.f : 1.f;
#pragma unroll
            for (int t = 0; t < 7; ++t)
#pragma unroll
                for (int s2 = 0; s2 < 2; ++s2) *(bf16x8*)(dst + (size_t)(14 * ch + 2 * t + s2) * FR) = pack8(acc[t], s2, sc);
        }
    }
    TL(256 + b, 1);
    {
        u32x4 t[2];
#pragma unroll
        for (int i = 0; i < 2; ++i) { const int e = min(b * 256 + tid + 65536 * i, NSEG * 28 * 64 - 1), ln = e & 63, ks = (e >> 6) % 28, seg = e / (28 * 64);
            t[i] = *(const u32x4*)(p.x + (size_t)(32 * seg + pi32(ln & 31)) * D + 16 * ks + 8 * (ln >> 5)); }
#pragma unroll
        for (int i = 0; i < 2; ++i) { const int e = b * 256 + tid + 65536 * i; if (e < NSEG * 28 * 64) *(u32x4*)(ws + WS_XP + (size_t)e * 16) = t[i]; }
    }
    for (int e = b * 256 + tid; e < NSEG * 2 * 1372; e += 65536) {
        const int pc = e % 1372, sp = e / 1372, st = sp >> 1, par2 = sp & 1;
        const unsigned in = pc < 448 ? pc * 16 : pc < 832 ? OFF_SC + (pc - 448) * 16 : pc < 1344 ? OFF_ET + (pc - 832) * 16 : OFF_FLG + (pc - 1344) * 128;
        const unsigned off = st * STEPB + par2 * 8192 + in;
        if (st == 0 && pc < 448) {
            const int G = pc >> 4, r = pc & 15, i = r >> 1, par = r & 1;
            u16 v[8];
#pragma unroll
            for (int j = 0; j < 8; ++j) v[j] = p.mem0[i * D + 16 * G + 4 * (j >> 1) + (j & 1) + 2 * par];
            u32x4 o = {v[0] | (unsigned)v[1] << 16, v[2] | (unsigned)v[3] << 16, v[4] | (unsigned)v[5] << 16, v[6] | (unsigned)v[7] << 16};
            o.x = nosent(o.x); o.y = nosent(o.y); o.z = nosent(o.z); o.w = nosent(o.w);
            *(u32x4*)(ws + WS_HAND + off) = o;
        } else *(u32x4*)(ws + WS_HAND + off) = (u32x4){SENT, SENT, SENT, SENT};
    }
    TL(256 + b, 2);
    if (b == 1 && tid < C_N) *(unsigned*)(ws + WS_CNT + (size_t)tid * 256) = 0u;
    if (b == 0) {
        if (tid < 32) {
            const int role = tid, h = role >> 1, beta = role & 1, ct = role < 28 ? role : 27;
            ChainBlock* cb = (ChainBlock*)(ws + WS_BLK) + role;
            auto desc = [&](unsigned* d, size_t off, unsigned bytes) { const unsigned long long a = (unsigned long long)(ws + off); d[0] = (unsigned)a; d[1] = (unsigned)(a >> 32) & 0xffffu; d[2] = bytes; d[3] = 0x00020000u; };
            desc(cb->rH, WS_HAND, NSEG * STEPB + STEPB); desc(cb->rVT, WS_VT, NSEG * 28 * H * 1024); desc(cb->rUM, WS_UM, NSEG * 4 * 32768); desc(cb->rVM, WS_VM, NSEG * 28 * 4096);
            desc(cb->rWA, WS_WAF, H * 28 * FR); desc(cb->rWB, WS_WBF, H * 28 * FR);
            cb->cnt = (unsigned long long)(ws + WS_CNT + C_CHAIN * 256); cb->til = (unsigned long long)(ws + WS_CNT + C_TILE * 256);
            cb->dbg = (unsigned long long)(ws + WS_DBG + (size_t)role * 131072); cb->pad0 = 0;
            cb->s = 0; cb->hb = 0; cb->hbn = STEPB; cb->vto = ct * 16 * 1024; cb->umo = (h >> 2) * 32768; cb->vmo = ct * 4096;
            cb->dA = CH_DA; cb->tmp = 0; cb->tmp2 = 0; cb->col = role < 28; cb->steps = NSEG; cb->dB = CH_DB; cb->par = 0; cb->pad1 = 0;
            unsigned long long n8 = 0;
            for (int l = 0; l < 64; ++l) if ((l & 15) < 8) n8 |= 1ull << l;
            cb->mN8 = n8; cb->mLO32 = 0xffffffffull;
            cb->fail = 0; cb->wao = h * 28 * FR + 7 * beta * 1024; cb->wbo = (ct >> 1) * 1024; cb->pad2 = 0;
            if (role == 0) *(unsigned*)(ws + WS_CNT + (size_t)C_PAR * 256 + 64) = SENT;
        }
    }
}
static_assert(C_PAR < C_N, "");

constexpr int L_RING = 0, RING_ST = 28672, L_WORK_END = 4 * RING_ST + 32768;

template <int OFF> DI void glds16(const char* base, unsigned voff, unsigned lds_dst) {
    unsigned keep;
    asm volatile("s_mov_b32 %0, m0\n\ts_mov_b32 m0, %3\n\ts_nop 0\n\tglobal_load_lds_dwordx4 %1, %2 offset:%c4\n\ts_mov_b32 m0, %0"
                 : "=&s"(keep) : "v"(voff), "s"(base), "s"(lds_dst), "i"(OFF) : "memory");
}
template <int NKS, bool W_IS_A, class LX>
DI void wgemm(const char* Wf, bf16x8 (&xf)[NKS], f32x16 (&acc)[14], char* lds, int w, int lane, LX&& loadx, bool first = false) {
    constexpr int NST = NKS / 2, NF = 14 * NKS, PF = 3;
    const char* wsrc = Wf + 7 * w * 1024;
    asm volatile("" : "+s"(wsrc));
    const unsigned voff = lane * 16, ldw = (unsigned)(L_RING + 7 * w * 1024);
    auto piece = [&](int st, int i) {
        const char* g = wsrc + (size_t)st * RING_ST + (i & 4) * 1024;
        const unsigned d = ldw + (st & 3) * RING_ST + (i & 4) * 1024;
        if ((i & 3) == 0) glds16<0>(g, voff, d); else if ((i & 3) == 1) glds16<1024>(g, voff, d); else if ((i & 3) == 2) glds16<2048>(g, voff, d); else glds16<3072>(g, voff, d);
    };
#pragma unroll
    for (int t = 0; t < 14; ++t)
#pragma unroll
        for (int i = 0; i < 16; ++i) acc[t][i] = 0.f;
    asm volatile("s_waitcnt vmcnt(0)" ::: "memory");
    BARRIER();
#pragma unroll
    for (int st = 0; st < 3; ++st)
#pragma unroll
        for (int i = 0; i < 7; ++i) piece(st, i);
    loadx();
#pragma unroll
    for (int k = 0; k < NKS; ++k) asm volatile("" : "+v"(xf[k]));
    TLF(11);
    asm volatile("s_waitcnt vmcnt(7)" ::: "memory");
    BARRIER();
    TLF(12);
    const char* fb = lds + L_RING + lane * 16;
    auto rd = [&](int f) { const int st = f / 28; return *(LAS bf16x8*)(fb + (st & 3) * RING_ST + (f % 28) * 1024); };
    bf16x8 wf[PF + 1];
#pragma unroll
    for (int f = 0; f < PF; ++f) wf[f] = rd(f);
    static_for<0, NST>([&](auto ST) {
        static_for<0, 28>([&](auto R) {
            constexpr int st = ST.value, r = R.value, f = 28 * st + r, k = f / 14, t = f % 14;
            if constexpr (r == 0 && st > 0) {
                if constexpr (st + 2 < NST) asm volatile("s_waitcnt vmcnt(7)" ::: "memory"); else asm volatile("s_waitcnt vmcnt(0)" ::: "memory");
                __builtin_amdgcn_s_barrier();
                asm volatile("" ::: "memory");
            }
            if constexpr (f + PF < NF) wf[(f + PF) % (PF + 1)] = rd(f + PF);
            if constexpr (W_IS_A) acc[t] = MFMA32(wf[f % (PF + 1)], xf[k], acc[t]); else acc[t] = MFMA32(xf[k], wf[f % (PF + 1)], acc[t]);
            if constexpr (st + 3 < NST && (r & 3) == 1) piece(st + 3, r >> 2);
            SCHED();
        });
    });
}
DI void load_x(const char* ws, int seg, int lane, bf16x8 (&xf)[28]) {
    const char* r = ws + WS_XP + (size_t)seg * 28672 + lane * 16;
#pragma unroll
    for (int k = 0; k < 28; ++k) xf[k] = *(const bf16x8*)(r + k * 1024);
}

DI void wait_count_acq(char* ws, int i, unsigned target) {
    if (threadIdx.x < 64) {
#pragma clang loop unroll(disable)
        for (unsigned spins = 0; spins < (1u << 20); ++spins) {
            const unsigned v = __hip_atomic_load(cntp(ws, i), __ATOMIC_RELAXED, __HIP_MEMORY_SCOPE_AGENT);
            if (__builtin_amdgcn_readfirstlane(v) >= target) break;
            __builtin_amdgcn_s_sleep(8);
        }
        __builtin_amdgcn_fence(__ATOMIC_ACQUIRE, "agent");
        asm volatile("s_waitcnt vmcnt(0)" ::: "memory");
    }
    BARRIER();
}
template <int N> DI void stage16(const u16* src, char* dst, int tid) {
    constexpr int IT = (N + 255) / 256;
    u32x4 t[IT];
#pragma unroll
    for (int i = 0; i < IT; ++i) t[i] = *(const u32x4*)(src + (size_t)min(tid + 256 * i, N - 1) * 8);
#pragma unroll
    for (int i = 0; i < IT; ++i) if (tid + 256 * i < N) *(LAS u32x4*)(dst + (tid + 256 * i) * 16) = t[i];
}
#define CHAIN_MAP 0
#define HELP_POLL 2
#define VT_PREFETCH 0
#define VT_PF_AT 1
#define HELP_SLEEP 0
#define CAL_N 10
DI void aux_role(int id, char* lds) {
    KParams& p = *kparams();
    char* ws = p.ws;
    const int tid = threadIdx.x, lane = tid & 63, w = __builtin_amdgcn_readfirstlane(tid >> 6);
    const rsrc_t rW = mkrs(ws + WS_WQF, 2 * MB), rX = mkrs(ws + WS_XF, NSEG * 32768), rG = mkrs(ws + WS_G, S * 4);
    if (id == 0) {
        GAS unsigned* a = (GAS unsigned*)(ws + WS_HAND + (size_t)(w >> 1) * 32 * STEPB + OFF_CAL + (w & 1) * 8192);
        unsigned best = ~0u;
        for (int it = 0; it < CAL_N; ++it) {
            const unsigned t0 = (unsigned)__builtin_amdgcn_s_memtime();
            if (lane == 0) __hip_atomic_store(a, (unsigned)it, __ATOMIC_RELAXED, __HIP_MEMORY_SCOPE_AGENT);
            asm volatile("s_waitcnt vmcnt(0)" ::: "memory");
            best = min(best, (unsigned)__builtin_amdgcn_s_memtime() - t0);
        }
        if (lane == 0) *(LAS unsigned*)(lds + w * 4) = best;
        BARRIER();
        if (tid == 0) { const u32x4 t = *(LAS u32x4*)lds;
            __hip_atomic_store((GAS unsigned*)(ws + WS_CNT + (size_t)C_PAR * 256 + 64), (t.y < t.x ? 0x2000u : 0u) | (t.w < t.z ? 0x20000000u : 0u), __ATOMIC_RELAXED, __HIP_MEMORY_SCOPE_AGENT); }
    }
    if (id < 14) {
        char* rows = lds;
        char* res = lds + 86080;
        const int m = lane & 31, kh = lane >> 5;
        BARRIER();
        stage16<32 * 168>(p.wqkv + (size_t)32 * id * 1344, rows, tid);
        if (tid < 4) *(LAS u32x4*)(rows + 32 * 2688 + tid * 16) = (u32x4){0, 0, 0, 0};
        char* phi = res;
        stage16<H * 28 * 28 / 8>(p.wphi, phi, tid);
        BARRIER();
        bf16x8 ph[4][2];
#pragma unroll
        for (int q = 0; q < 4; ++q)
#pragma unroll
            for (int s1 = 0; s1 < 2; ++s1) {
                unsigned v[8];
#pragma unroll
                for (int j = 0; j < 8; ++j) { const int e = 16 * s1 + 8 * kh + j; const unsigned x = *(LAS u16*)(phi + (((4 * w + q) * 28 + min(e, 27)) * 28 + min(m, 27)) * 2); v[j] = (e < 28 && m < 28) ? x : 0u; }
                ph[q][s1] = asfrag((u32x4){v[0] | v[1] << 16, v[2] | v[3] << 16, v[4] | v[5] << 16, v[6] | v[7] << 16});
            }
        BARRIER();
#pragma unroll
        for (int q = 0; q < 4; ++q)
#pragma unroll
            for (int ty = 0; ty < 2; ++ty) {
                const int h = 4 * w + q;
                f32x16 d;
#pragma unroll
                for (int i = 0; i < 16; ++i) d[i] = 0.f;
#pragma unroll
                for (int s1 = 0; s1 < 2; ++s1) {
                    const char* a = rows + m * 2688 + ty * 896 + h * 56 + (16 * s1 + 8 * kh) * 2;
                    const u32x2 lo = *(LAS u32x2*)a, hi = *(LAS u32x2*)(a + 8);
                    d = MFMA32(ph[q][s1], asfrag((u32x4){lo.x, lo.y, hi.x, hi.y}), d);
                }
#pragma unroll
                for (int r = 0; r < 16; ++r) { const int f = (r & 3) + 8 * (r >> 2) + 4 * kh;
                    if (f < 28) *(LAS u16*)(res + m * 1792 + ty * 896 + (h * 28 + f) * 2) = __builtin_bit_cast(u16, (__bf16)d[r]); }
            }
        BARRIER();
#pragma unroll 3
        for (int e = tid; e < 2 * 3 * 896; e += 256) {
            const int s1 = e / 2688, im = (e % 2688) / 896, pc = e % 896, ln = pc & 63, n = 32 * (pc >> 6) + pi32(ln & 31), c0 = 16 * s1 + 8 * (ln >> 5);
            const char* src = im < 2 ? res + im * 896 + n * 2 : rows + 1792 + n * 2; const int st = im < 2 ? 1792 : 2688;
            unsigned v[8];
#pragma unroll
            for (int j = 0; j < 8; ++j) v[j] = *(LAS u16*)(src + (c0 + j) * st);
            st16c(rW, (unsigned)(im * (MB / 2) + ((2 * id + s1) * 896 + pc) * 16), (u32x4){v[0] | v[1] << 16, v[2] | v[3] << 16, v[4] | v[5] << 16, v[6] | v[7] << 16});
        }
        signal_done(ws, C_AUXW);
    }
    TL(blockIdx.x, 1);
#pragma unroll 1
    for (int seg = 4 * id; seg < 4 * id + 4; ++seg) {
        u32x4 t[8], xg[7], gg[7];
#pragma unroll
        for (int i = 0; i < 8; ++i) { const int e = tid + 256 * i, ln = e & 63, u = e >> 6, n = ln & 31, kh = ln >> 5, be = u >> 4, o = 16 * (u & 15) + 8 * kh;
            t[i] = *(const u32x4*)(p.x + (size_t)(32 * seg + n) * D + 224 * be + (o < 224 ? o : o - 32)); }
        const int tk = 32 * seg + (tid >> 3), c = tid & 7;
#pragma unroll
        for (int i = 0; i < 7; ++i) { xg[i] = *(const u32x4*)(p.x + (size_t)tk * D + 8 * (c + 8 * i)); gg[i] = *(const u32x4*)(p.gw + 8 * (c + 8 * i)); }
#pragma unroll
        for (int i = 0; i < 8; ++i) st16c(rX, (unsigned)(seg * 32768 + (tid + 256 * i) * 16), t[i]);
        float a = 0.f;
#pragma unroll
        for (int i = 0; i < 7; ++i)
#pragma unroll
            for (int j = 0; j < 4; ++j) a += rbf(blo(xg[i][j]) * blo(gg[i][j])) + rbf(bhi(xg[i][j]) * bhi(gg[i][j]));
        a += __shfl_xor(a, 1); a += __shfl_xor(a, 2); a += __shfl_xor(a, 4);
        a = rbf(a);
        if (c == 0) st4c(rG, (unsigned)(tk * 4), __float_as_uint(rbf(1.f / (1.f + __expf(-a)))));
    }
    signal_done(ws, C_AUXX);
    TL(blockIdx.x, 2);
    BARRIER();
    stage16<28 * 56>(p.wo + (size_t)id * 28 * D, lds, tid);
    BARRIER();
    for (int e = tid; e < 2 * 14 * 64; e += 256) {
        const int ln = e & 63, tile = (e >> 6) % 14, s1 = e / (64 * 14), m = ln & 31, kh = ln >> 5, d = 32 * tile + pi32(m);
        unsigned v[8];
#pragma unroll
        for (int j = 0; j < 8; ++j) { const int ee = 16 * s1 + 8 * kh + j; v[j] = ee < 28 ? *(LAS u16*)(lds + (ee * D + d) * 2) : 0u; }
        st16c(rW, (unsigned)(3 * (MB / 2) + ((2 * id + s1) * 896 + (e % 896)) * 16), (u32x4){v[0] | v[1] << 16, v[2] | v[3] << 16, v[4] | v[5] << 16, v[6] | v[7] << 16});
    }
    signal_done(ws, C_AUXO);
    TL(blockIdx.x, 3);
}

template <int CLS>
DI void proj_task(int type, int h, int tile, char* lds, bool first = false) {
    KParams& p = *kparams();
    char* ws = p.ws;
    const int tid = opaque_tid(), lane = tid & 63, w = __builtin_amdgcn_readfirstlane(tid >> 6), n = lane & 31, hh = lane >> 5;
    const int seg = 4 * tile + w, tk = pi32(n);
    if (CLS == 2) wait_count_acq(ws, C_AUXW, 14);
    bf16x8 xf[28];
    TLF(10);
    f32x16 acc[14];
    const char* Wf = ws + (CLS == 0 ? WS_WAF + (size_t)h * 28 * FR : CLS == 1 ? WS_WBF + (size_t)h * 28 * FR : type == 2 ? WS_WQF : type == 3 ? WS_WKF : WS_WVF);
    wgemm<28, CLS != 1>(Wf, xf, acc, lds, w, lane, [&]() { load_x(ws, seg, lane, xf); }, first);
    TLF(13);
    if (CLS == 0) {
        const rsrc_t rU = mkrs(ws + WS_UT, NSEG * H * 28672), rE = mkrs(ws + WS_ETT, NSEG * H * 2048), rM = mkrs(ws + WS_MLT, NSEG * H * 256);
        f32x16 sc;
#pragma unroll
        for (int i = 0; i < 16; ++i) sc[i] = 0.f;
#pragma unroll
        for (int t = 0; t < 14; ++t)
#pragma unroll
            for (int s2 = 0; s2 < 2; ++s2) {
                const bf16x8 uf = pack8(acc[t], s2, 1.f);
                const int o8 = 4 * t + 2 * s2 + hh;
                st16c(rU, (unsigned)(((((seg * H + h) * 2 + (tk >> 4)) * 7 + (o8 >> 3)) * 2 + ((o8 >> 2) & 1)) * 1024 + ((o8 & 3) * 16 + (tk & 15)) * 16), __builtin_bit_cast(u32x4, uf));
                sc = MFMA32(uf, xf[2 * t + s2], sc);
            }
        float m = -3.0e38f;
#pragma unroll
        for (int r = 0; r < 16; ++r) { const int j = 16 * (r >> 3) + 8 * hh + (r & 7); if (j > tk) sc[r] = -3.0e38f; m = fmaxf(m, sc[r]); }
        m = xhalf_max(m);
        float l = 0.f;
#pragma unroll
        for (int r = 0; r < 16; ++r) { const int j = 16 * (r >> 3) + 8 * hh + (r & 7); sc[r] = j > tk ? 0.f : __builtin_amdgcn_exp2f(sc[r] - m); l += sc[r]; }
        l = xhalf_sum(l);
#pragma unroll
        for (int s2 = 0; s2 < 2; ++s2) st16c(rE, (unsigned)(((seg * H + h) * 32 + tk) * 64 + (2 * s2 + hh) * 16), __builtin_bit_cast(u32x4, pack8(sc, s2, 1.f)));
        if (hh == 0) st8c(rM, (unsigned)(((seg * H + h) * 32 + tk) * 8), (u32x2){__float_as_uint(m), __float_as_uint(l)});
        TLF(14);
        signal_done(ws, C_TILE + tile, tile < 2 ? 0u : 5u, C_ETT + tile);
        TLF(15);
    } else if (CLS == 1) {
        const rsrc_t rV = mkrs(ws + WS_VT, NSEG * 28 * H * 1024);
#pragma unroll
        for (int t = 0; t < 14; ++t)
#pragma unroll
            for (int s2 = 0; s2 < 2; ++s2)
                st16c(rV, (unsigned)(((seg * 28 + 2 * t + (n >> 4)) * H + h) * 1024 + ((2 * s2 + hh) * 16 + (n & 15)) * 16), __builtin_bit_cast(u32x4, pack8(acc[t], s2, 1.f)));
        signal_done(ws, C_TILE + tile, 5u);
    } else {
        const rsrc_t rO = mkrs(ws + (type == 2 ? WS_PQ : type == 3 ? WS_PK : WS_V), S * D * 2);
#pragma unroll
        for (int t = 0; t < 14; ++t)
#pragma unroll
            for (int s2 = 0; s2 < 2; ++s2)
                st16c(rO, (unsigned)(((32 * seg + tk) * D + 32 * t + 16 * s2 + 8 * hh) * 2), __builtin_bit_cast(u32x4, pack8(acc[t], s2, 1.f)));
        signal_done(ws, (type == 2 ? C_PQ : type == 3 ? C_PK : C_V) + tile);
    }
}

template <int TYPE>
DI void fast_task(int h, int part, char* lds) {
    char* ws = kparams()->ws;
    const int tid = opaque_tid(), lane = tid & 63, w = __builtin_amdgcn_readfirstlane(tid >> 6), n = lane & 31, hh = lane >> 5, tk = pi32(n);
    const int t0 = 3 * part, nt = min(3, 14 - t0);
    const char* Wf = ws + (TYPE == 0 ? WS_WAF : WS_WBF) + (size_t)h * 28 * FR + t0 * 1024;
    asm volatile("" : "+s"(Wf));
#pragma unroll
    for (int i = 0; i < 7; ++i) { const int ks = 7 * w + i; const char* g = Wf + (size_t)ks * FR; const unsigned d = (unsigned)(ks * 3072);
        glds16<0>(g, lane * 16, d); glds16<1024>(g, lane * 16, d); glds16<2048>(g, lane * 16, d); }
    const rsrc_t rU = mkrs(ws + WS_UT, NSEG * H * 28672), rV = mkrs(ws + WS_VT, NSEG * 28 * H * 1024);
#pragma unroll 1
    for (int tile = 0; tile < 2; ++tile) {
        const int seg = 4 * tile + w;
        bf16x8 xf[28];
        load_x(ws, seg, lane, xf);
#pragma unroll
        for (int k = 0; k < 28; ++k) asm volatile("" : "+v"(xf[k]));
        TL(blockIdx.x, 16 + 4 * tile);
        if (tile == 0) { asm volatile("s_waitcnt vmcnt(0)" ::: "memory"); BARRIER(); }
        TL(blockIdx.x, 17 + 4 * tile);
        f32x16 acc[3];
#pragma unroll
        for (int t = 0; t < 3; ++t)
#pragma unroll
            for (int i = 0; i < 16; ++i) acc[t][i] = 0.f;
        {
            const char* fb = lds + lane * 16;
            bf16x8 wf[4];
#pragma unroll
            for (int f = 0; f < 3; ++f) wf[f] = *(LAS bf16x8*)(fb + f * 1024);
            static_for<0, 84>([&](auto F) {
                constexpr int f = F.value;
                if constexpr (f + 3 < 84) wf[(f + 3) & 3] = *(LAS bf16x8*)(fb + (f + 3) * 1024);
                if constexpr (TYPE == 0) acc[f % 3] = MFMA32(wf[f & 3], xf[f / 3], acc[f % 3]); else acc[f % 3] = MFMA32(xf[f / 3], wf[f & 3], acc[f % 3]);
                SCHED();
            });
        }
        TL(blockIdx.x, 18 + 4 * tile);
#pragma unroll
        for (int t = 0; t < 3; ++t)
            if (t < nt) {
#pragma unroll
                for (int s2 = 0; s2 < 2; ++s2) {
                    const u32x4 v = __builtin_bit_cast(u32x4, pack8(acc[t], s2, 1.f));
                    if (TYPE == 0) { const int o8 = 4 * (t0 + t) + 2 * s2 + hh;
                        st16c(rU, (unsigned)(((((seg * H + h) * 2 + (tk >> 4)) * 7 + (o8 >> 3)) * 2 + ((o8 >> 2) & 1)) * 1024 + ((o8 & 3) * 16 + (tk & 15)) * 16), v); }
                    else st16c(rV, (unsigned)(((seg * 28 + 2 * (t0 + t) + (n >> 4)) * H + h) * 1024 + ((2 * s2 + hh) * 16 + (n & 15)) * 16), v);
                }
            }
        signal_done(ws, C_TILE + tile);
        TL(blockIdx.x, 19 + 4 * tile);
    }
    BARRIER();
}

DI void feature_map(const float (&pv)[28], float (&fp)[28], float (&fm)[28]) {
    float mx = pv[0], mn = pv[0];
#pragma unroll
    for (int i = 1; i < 28; ++i) { mx = fmaxf(mx, pv[i]); mn = fminf(mn, pv[i]); }
    float sp = 0.f, sm = 0.f;
#pragma unroll
    for (int i = 0; i < 28; ++i) { fp[i] = __builtin_amdgcn_exp2f((pv[i] - mx) * LOG2E); fm[i] = __builtin_amdgcn_exp2f((mn - pv[i]) * LOG2E); sp += fp[i]; sm += fm[i]; }
    const float rp = __builtin_amdgcn_rcpf(sp), rm = __builtin_amdgcn_rcpf(sm);
#pragma unroll
    for (int i = 0; i < 28; ++i) { fp[i] *= rp; fm[i] *= rm; }
}
DI void load28(rsrc_t r, unsigned off, float (&v)[28]) {
#pragma unroll
    for (int i = 0; i < 7; ++i) { const u32x2 q = ld8c(r, off + 8 * i); v[4 * i] = blo(q.x); v[4 * i + 1] = bhi(q.x); v[4 * i + 2] = blo(q.y); v[4 * i + 3] = bhi(q.y); }
}

DI void kv_task(int h, int chunk, char* lds) {
    KParams& p = *kparams();
    char* ws = p.ws;
    const int tid = opaque_tid(), lane = tid & 63, w = __builtin_amdgcn_readfirstlane(tid >> 6);
    for (int t = 4 * chunk; t < 4 * chunk + 4; ++t) { wait_count(ws, C_PK + t, 1); wait_count(ws, C_V + t, 1); }
    const rsrc_t rK = mkrs(ws + WS_PK, S * D * 2), rV = mkrs(ws + WS_V, S * D * 2), rP = mkrs(ws + WS_KVP, H * 4 * 8192);
    char* PH = lds + w * 12288; char* VI = PH + 8192;
    f32x16 kp, km;
#pragma unroll
    for (int i = 0; i < 16; ++i) { kp[i] = 0.f; km[i] = 0.f; }
    const int blk = (lane >> 4) & 1, kh = lane >> 5, qq = (lane >> 2) & 3, pp = lane & 3;
    for (int it = 0; it < 2; ++it) {
        const int tok = 512 * chunk + 128 * w + 64 * it + lane;
        float pv[28], vv[28], fp[28], fm[28];
        load28(rK, (unsigned)((tok * D + 28 * h) * 2), pv);
        load28(rV, (unsigned)((tok * D + 28 * h) * 2), vv);
        feature_map(pv, fp, fm);
#pragma unroll
        for (int c = 0; c < 4; ++c) {
            u32x4 a, bq, v4;
#define PK2(arr, i) cvtpk((i) < 28 ? arr[(i) < 28 ? (i) : 0] : 0.f, (i) + 1 < 28 ? arr[(i) + 1 < 28 ? (i) + 1 : 0] : 0.f)
            a = (u32x4){PK2(fp, 8 * c), PK2(fp, 8 * c + 2), PK2(fp, 8 * c + 4), PK2(fp, 8 * c + 6)};
            bq = (u32x4){PK2(fm, 8 * c), PK2(fm, 8 * c + 2), PK2(fm, 8 * c + 4), PK2(fm, 8 * c + 6)};
            v4 = (u32x4){PK2(vv, 8 * c), PK2(vv, 8 * c + 2), c == 3 ? cvtpk(1.f, 0.f) : PK2(vv, 8 * c + 4), PK2(vv, 8 * c + 6)};
            *(LAS u32x4*)(PH + lane * 128 + c * 16) = a; *(LAS u32x4*)(PH + lane * 128 + 64 + c * 16) = bq; *(LAS u32x4*)(VI + lane * 64 + c * 16) = v4;
        }
        asm volatile("s_waitcnt lgkmcnt(0)" ::: "memory");
#pragma unroll
        for (int u = 0; u < 4; ++u) {
            const int row = 16 * u + 8 * kh + qq;
            auto tr2 = [&](const char* a, int stride4) { const bf16x4 lo = __builtin_amdgcn_ds_read_tr16_b64_v4bf16((LAS bf16x4*)a), hi = __builtin_amdgcn_ds_read_tr16_b64_v4bf16((LAS bf16x4*)(a + stride4));
                return (bf16x8)__builtin_shufflevector(lo, hi, 0, 1, 2, 3, 4, 5, 6, 7); };
            const bf16x8 ap = tr2(PH + row * 128 + blk * 32 + pp * 8, 512), am = tr2(PH + row * 128 + 64 + blk * 32 + pp * 8, 512), bv = tr2(VI + row * 64 + blk * 32 + pp * 8, 256);
            kp = MFMA32(ap, bv, kp); km = MFMA32(am, bv, km);
        }
        asm volatile("s_waitcnt lgkmcnt(0)" ::: "memory");
    }
    BARRIER();
    float* red = (float*)lds;
    {
        const int e = lane & 31;
#pragma unroll
        for (int r = 0; r < 16; ++r) { const int f = (r & 3) + 8 * (r >> 2) + 4 * kh;
            *(LAS float*)(lds + ((w * 64 + f) * 32 + e) * 4) = kp[r]; *(LAS float*)(lds + ((w * 64 + 32 + f) * 32 + e) * 4) = km[r]; }
    }
    (void)red;
    BARRIER();
    {
        f32x4 a[2];
#pragma unroll
        for (int i = 0; i < 2; ++i) { const int idx = (tid + 256 * i) * 16;
            a[i] = (*(LAS f32x4*)(lds + idx) + *(LAS f32x4*)(lds + 8192 + idx)) + (*(LAS f32x4*)(lds + 16384 + idx) + *(LAS f32x4*)(lds + 24576 + idx));
            st16c(rP, (unsigned)((h * 4 + chunk) * 8192 + idx), __builtin_bit_cast(u32x4, a[i])); }
    }
    asm volatile("s_waitcnt vmcnt(0)" ::: "memory");
    BARRIER();
    if (tid == 0) *(LAS unsigned*)(lds + 32768) = __hip_atomic_fetch_add(cntp(ws, C_KVT + h), 1u, __ATOMIC_RELAXED, __HIP_MEMORY_SCOPE_AGENT);
    BARRIER();
    if (*(LAS unsigned*)(lds + 32768) == 3) {
        const int ks = tid >> 6, m = lane & 31, e = pi32(m);
        float v[8];
#pragma unroll
        for (int j = 0; j < 8; ++j) { const int f = 16 * ks + 8 * kh + j; float a = 0.f;
#pragma unroll
            for (int c = 0; c < 4; ++c) a += __uint_as_float(__builtin_amdgcn_raw_buffer_load_b32(rP, (unsigned)((h * 4 + c) * 8192 + (f * 32 + e) * 4), 0, AUX_SC1));
            v[j] = a; }
        const rsrc_t rF = mkrs(ws + WS_KVF, H * 4096);
        st16c(rF, (unsigned)(h * 4096 + ks * 1024 + lane * 16), (u32x4){cvtpk(v[0], v[1]), cvtpk(v[2], v[3]), cvtpk(v[4], v[5]), cvtpk(v[6], v[7])});
        signal_done(ws, C_KV);
    }
    BARRIER();
}

DI void outa_task(int tile, char* lds) {
    KParams& p = *kparams();
    char* ws = p.ws;
    const int tid = opaque_tid(), lane = tid & 63, w = __builtin_amdgcn_readfirstlane(tid >> 6), n = lane & 31, hh = lane >> 5;
    wait_count(ws, C_PQ + tile, 1);
    const int tok = 128 * tile + 32 * w + n;
    const rsrc_t rQ = mkrs(ws + WS_PQ, S * D * 2), rF = mkrs(ws + WS_KVF, H * 4096), rG = mkrs(ws + WS_GA, S * D * 4);
#pragma unroll
    for (int c = 0; c < 4; ++c) {
        u32x4 t[7];
#pragma unroll
        for (int i = 0; i < 7; ++i) { const int e = tid + 256 * (7 * c + i); t[i] = ld16c(rQ, (unsigned)(128 * tile * 896 + e * 16)); }
#pragma unroll
        for (int i = 0; i < 7; ++i) { const int e = tid + 256 * (7 * c + i), r = e / 56, k = e % 56; *(LAS u32x4*)(lds + r * 912 + k * 16) = t[i]; }
    }
    wait_count(ws, C_KV, 16); wait_count(ws, C_AUXX, 16); wait_count_acq(ws, C_AUXO, 16);
    const char* row = lds + (32 * w + n) * 912;
    bf16x8 of[32];
    u32x4 kf[2][4];
#pragma unroll
    for (int ks = 0; ks < 4; ++ks) kf[0][ks] = ld16c(rF, (unsigned)(ks * 1024 + lane * 16));
    static_for<0, H>([&](auto HH) {
        constexpr int h = HH.value;
        if constexpr (h + 1 < H) {
#pragma unroll
            for (int ks = 0; ks < 4; ++ks) kf[(h + 1) & 1][ks] = ld16c(rF, (unsigned)((h + 1) * 4096 + ks * 1024 + lane * 16));
        }
        float pv[28];
        const float sg = hh ? -1.f : 1.f;
#pragma unroll
        for (int i = 0; i < 7; ++i) { const u32x2 q = *(LAS u32x2*)(row + h * 56 + i * 8); pv[4 * i] = sg * blo(q.x); pv[4 * i + 1] = sg * bhi(q.x); pv[4 * i + 2] = sg * blo(q.y); pv[4 * i + 3] = sg * bhi(q.y); }
        float mx = pv[0];
#pragma unroll
        for (int i = 1; i < 28; ++i) mx = fmaxf(mx, pv[i]);
        float sm = 0.f;
#pragma unroll
        for (int i = 0; i < 28; ++i) { pv[i] = __builtin_amdgcn_exp2f((pv[i] - mx) * LOG2E); sm += pv[i]; }
        const float rs = __builtin_amdgcn_rcpf(sm);
        unsigned pk[16];
#pragma unroll
        for (int i = 0; i < 16; ++i) pk[i] = i < 14 ? cvtpk(pv[2 * (i < 14 ? i : 0)] * rs, pv[2 * (i < 14 ? i : 0) + 1] * rs) : 0u;
        f32x16 nu;
#pragma unroll
        for (int i = 0; i < 16; ++i) nu[i] = 0.f;
#pragma unroll
        for (int s1 = 0; s1 < 2; ++s1) {
            u32x4 fpl, fmi;
#pragma unroll
            for (int j = 0; j < 4; ++j) { const auto sw2 = __builtin_amdgcn_permlane32_swap(pk[8 * s1 + j], pk[8 * s1 + 4 + j], false, false); fpl[j] = sw2[0]; fmi[j] = sw2[1]; }
            nu = MFMA32(asfrag(kf[h & 1][s1]), asfrag(fpl), nu);
            nu = MFMA32(asfrag(kf[h & 1][2 + s1]), asfrag(fmi), nu);
        }
        auto sw = __builtin_amdgcn_permlane32_swap(__float_as_uint(nu[12]), __float_as_uint(nu[12]), false, false);
        const float den = rbf(rbf(__uint_as_float(sw[1])) + 1e-6f);
        const float rd = __builtin_amdgcn_rcpf(den);
        f32x16 o;
#pragma unroll
        for (int i = 0; i < 16; ++i) o[i] = rbf(nu[i]) * rd;
        of[2 * h] = pack8(o, 0, 1.f); of[2 * h + 1] = pack8(o, 1, 1.f);
        SCHED();
    });
    f32x16 acc[14];
    wgemm<32, true>(ws + WS_WOF, of, acc, lds, w, lane, []() {});
    const float g = __uint_as_float(__builtin_amdgcn_raw_buffer_load_b32(mkrs(ws + WS_G, S * 4), (unsigned)(tok * 4), 0, AUX_SC1));
#pragma unroll
    for (int t = 0; t < 14; ++t)
#pragma unroll
        for (int s2 = 0; s2 < 2; ++s2) {
            const unsigned off = (unsigned)((tok * D + 32 * t + 16 * s2 + 8 * hh) * 4);
            f32x4 a, c;
#pragma unroll
            for (int i = 0; i < 4; ++i) { a[i] = g * rbf(acc[t][8 * s2 + i]); c[i] = g * rbf(acc[t][8 * s2 + 4 + i]); }
            st16c(rG, off, __builtin_bit_cast(u32x4, a)); st16c(rG, off + 16, __builtin_bit_cast(u32x4, c));
        }
    signal_done(ws, C_GA + tile);
}

DI void tail_task(int seg, int cg, char* lds) {
    KParams& p = *kparams();
    char* ws = p.ws;
    const int tid = opaque_tid(), lane = tid & 63, w = __builtin_amdgcn_readfirstlane(tid >> 6), n = lane & 31, hh = lane >> 5;
    const rsrc_t rX = mkrs(ws + WS_XF, NSEG * 32768), rU = mkrs(ws + WS_UM, NSEG * 4 * 32768), rE = mkrs(ws + WS_ETT, NSEG * H * 2048), rM = mkrs(ws + WS_MLT, NSEG * H * 256);
    const rsrc_t rV = mkrs(ws + WS_VT, NSEG * 28 * H * 1024), rVM = mkrs(ws + WS_VM, NSEG * 28 * 4096), rG = mkrs(ws + WS_GA, S * D * 4);
    BARRIER();
    wait_count(ws, C_TILE + (seg >> 2), TILE_DONE); wait_count(ws, C_ETT + (seg >> 2), 16); wait_count(ws, C_GA + (seg >> 2), 1);
    const int dm = pi32(n);
    f32x16 ah[4][2];
    {
        u32x4 xs[8];
#pragma unroll
        for (int i = 0; i < 8; ++i) xs[i] = ld16c(rX, (unsigned)(seg * 32768 + (8 * w + i) * 1024 + lane * 16));
        u32x4 va[4][2][2], ev[4][2];
#pragma unroll
        for (int q = 0; q < 4; ++q)
#pragma unroll
            for (int u = 0; u < 2; ++u) {
                ev[q][u] = ld16c(rE, (unsigned)(((seg * H + 4 * w + q) * 32 + n) * 64 + (2 * u + hh) * 16));
#pragma unroll
                for (int nt = 0; nt < 2; ++nt) { const int d = 64 * cg + 32 * nt + dm;
                    va[q][nt][u] = ld16c(rV, (unsigned)(((seg * 28 + (d >> 4)) * H + 4 * w + q) * 1024 + ((2 * u + hh) * 16 + (d & 15)) * 16)); }
            }
        SCHED();
#pragma unroll
        for (int i = 0; i < 8; ++i) *(LAS u32x4*)(lds + (8 * w + i) * 1024 + lane * 16) = xs[i];
#pragma unroll
        for (int q = 0; q < 4; ++q)
#pragma unroll
            for (int nt = 0; nt < 2; ++nt) {
#pragma unroll
                for (int i = 0; i < 16; ++i) ah[q][nt][i] = 0.f;
#pragma unroll
                for (int u = 0; u < 2; ++u) ah[q][nt] = MFMA32(asfrag(va[q][nt][u]), asfrag(ev[q][u]), ah[q][nt]);
            }
    }
    u32x2 ml[4];
#pragma unroll
    for (int q = 0; q < 4; ++q) ml[q] = ld8c(rM, (unsigned)(((seg * H + 4 * w + q) * 32 + n) * 8));
    const int tke = tid >> 3, c8e = tid & 7;
    const unsigned gaoff = (unsigned)(((32 * seg + tke) * D + 64 * cg + 8 * c8e) * 4);
    const f32x4 ga = __builtin_bit_cast(f32x4, ld16c(rG, gaoff)), gc = __builtin_bit_cast(f32x4, ld16c(rG, gaoff + 16));
    const float g1 = rbf(1.f - __uint_as_float(__builtin_amdgcn_raw_buffer_load_b32(mkrs(ws + WS_G, S * 4), (unsigned)((32 * seg + tke) * 4), 0, AUX_SC1)));
    wait_count(ws, C_CHAIN + seg, 32, TAIL_SLEEP);
    f32x16 sc;
#pragma unroll
    for (int i = 0; i < 16; ++i) sc[i] = 0.f;
    u32x4 vm[2][2];
#pragma unroll
    for (int v = 0; v < 2; ++v)
#pragma unroll
        for (int nt = 0; nt < 2; ++nt) { const int d = 64 * cg + 32 * nt + dm;
            vm[v][nt] = ld16c(rVM, (unsigned)(((seg * 28 + (d >> 4)) * 4 + w) * 1024 + ((2 * v + hh) * 16 + (d & 15)) * 16)); }
#pragma unroll
    for (int c = 0; c < 2; ++c) {
        u32x4 um[16];
#pragma unroll
        for (int i = 0; i < 16; ++i) um[i] = ld16c(rU, (unsigned)(((seg * 4 + w) * 32 + 16 * c + i) * 1024 + lane * 16));
#pragma unroll
        for (int i = 0; i < 16; ++i) sc = MFMA32(asfrag(um[i]), *(LAS bf16x8*)(lds + (16 * c + i) * 1024 + lane * 16), sc);
    }
    float gt[4]; unsigned pk[4][2];
#pragma unroll
    for (int q = 0; q < 4; ++q) {
        const float mt = __uint_as_float(ml[q].x), lt = __uint_as_float(ml[q].y);
        float m = fmaxf(fmaxf(sc[4 * q], sc[4 * q + 1]), fmaxf(sc[4 * q + 2], sc[4 * q + 3]));
        m = fmaxf(xhalf_max(m), mt);
        float e[4], l = 0.f;
#pragma unroll
        for (int i = 0; i < 4; ++i) { e[i] = __builtin_amdgcn_exp2f(sc[4 * q + i] - m); l += e[i]; }
        const float f = __builtin_amdgcn_exp2f(mt - m);
        const float r = 1.f / (xhalf_sum(l) + f * lt);
        gt[q] = f * r;
        pk[q][0] = cvtpk(e[0] * r, e[1] * r); pk[q][1] = cvtpk(e[2] * r, e[3] * r);
    }
    f32x16 o[2];
#pragma unroll
    for (int nt = 0; nt < 2; ++nt)
#pragma unroll
        for (int i = 0; i < 16; ++i) o[nt][i] = (gt[0] * ah[0][nt][i] + gt[1] * ah[1][nt][i]) + (gt[2] * ah[2][nt][i] + gt[3] * ah[3][nt][i]);
#pragma unroll
    for (int v = 0; v < 2; ++v) {
        const auto s0 = __builtin_amdgcn_permlane32_swap(pk[2 * v][0], pk[2 * v + 1][0], false, false);
        const auto s1 = __builtin_amdgcn_permlane32_swap(pk[2 * v][1], pk[2 * v + 1][1], false, false);
        const bf16x8 pb = asfrag((u32x4){s0[0], s1[0], s0[1], s1[1]});
#pragma unroll
        for (int nt = 0; nt < 2; ++nt) o[nt] = MFMA32(asfrag(vm[v][nt]), pb, o[nt]);
    }
    char* red = lds + 32768;
#pragma unroll
    for (int nt = 0; nt < 2; ++nt)
#pragma unroll
        for (int s2 = 0; s2 < 2; ++s2) {
            char* a = red + (w * 32 + n) * 272 + (32 * nt + 16 * s2 + 8 * hh) * 4;
            *(LAS f32x4*)a = (f32x4){o[nt][8 * s2], o[nt][8 * s2 + 1], o[nt][8 * s2 + 2], o[nt][8 * s2 + 3]};
            *(LAS f32x4*)(a + 16) = (f32x4){o[nt][8 * s2 + 4], o[nt][8 * s2 + 5], o[nt][8 * s2 + 6], o[nt][8 * s2 + 7]};
        }
    BARRIER();
    {
        const int tk = tke, c8 = c8e, tok = 32 * seg + tk;
        f32x4 a = {0, 0, 0, 0}, c = {0, 0, 0, 0};
#pragma unroll
        for (int q = 0; q < 4; ++q) { const char* r = red + (q * 32 + tk) * 272 + c8 * 32; a += *(LAS f32x4*)r; c += *(LAS f32x4*)(r + 16); }
        u32x4 o4;
        o4.x = cvtpk(rbf(ga[0]) + rbf(g1 * rbf(a[0])), rbf(ga[1]) + rbf(g1 * rbf(a[1]))); o4.y = cvtpk(rbf(ga[2]) + rbf(g1 * rbf(a[2])), rbf(ga[3]) + rbf(g1 * rbf(a[3])));
        o4.z = cvtpk(rbf(gc[0]) + rbf(g1 * rbf(c[0])), rbf(gc[1]) + rbf(g1 * rbf(c[1]))); o4.w = cvtpk(rbf(gc[2]) + rbf(g1 * rbf(c[2])), rbf(gc[3]) + rbf(g1 * rbf(c[3])));
        *(u32x4*)(p.out + (size_t)tok * D + 64 * cg + 8 * c8) = o4;
    }
}

DI void helper_role(int h, char* lds) {
    if (threadIdx.x >= 128) return;
    char* ws = kparams()->ws;
    const int lane = threadIdx.x & 63, w = __builtin_amdgcn_readfirstlane(threadIdx.x >> 6), n = lane & 15, g = lane >> 4;
    if (w == 1) {
        const rsrc_t rUt = mkrs(ws + WS_UT, NSEG * H * 28672);
        for (int s = 0; s < NSEG - 1; ++s) {
            if ((s & 3) == 0) {
#pragma clang loop unroll(disable)
                for (unsigned spins = 0; spins < (1u << 20); ++spins) {
                    const unsigned v = __hip_atomic_load(cntp(ws, C_TILE + (s >> 2)), __ATOMIC_RELAXED, __HIP_MEMORY_SCOPE_AGENT);
                    if (__builtin_amdgcn_readfirstlane(v) >= TILE_DONE) break;
                    __builtin_amdgcn_s_sleep(4);
                }
                asm volatile("" ::: "memory");
            }
            u32x4 t[28];
#pragma unroll
            for (int k = 0; k < 28; ++k) t[k] = ld16c(rUt, (unsigned)((s * H + h) * 28672 + k * 1024 + lane * 16));
#pragma unroll
            for (int k = 0; k < 28; ++k) *(LAS u32x4*)(lds + (s & 1) * 28672 + k * 1024 + lane * 16) = t[k];
            BARRIER();
        }
        return;
    }
    const int idxM = (n & 1) ? 0xEEEE : 0x4444;
    const rsrc_t rh = mkrs(ws + WS_HAND, NSEG * STEPB + STEPB);
    unsigned par = SENT;
#pragma clang loop unroll(disable)
    for (unsigned spins = 0; spins < (1u << 20) && par == SENT; ++spins)
        par = __builtin_amdgcn_readfirstlane(__hip_atomic_load((GAS unsigned*)(ws + WS_CNT + (size_t)C_PAR * 256 + 64), __ATOMIC_RELAXED, __HIP_MEMORY_SCOPE_AGENT));
    if (par == SENT) par = 0;
    for (int s = 0; s < NSEG - 1; ++s) {
        const unsigned hb = (unsigned)s * STEPB + ((par >> (16 * (s >> 5))) & 0x2000u);
        BARRIER();
        bf16x16 Ut[2][7];
#pragma unroll
        for (int t = 0; t < 2; ++t)
#pragma unroll
            for (int k = 0; k < 7; ++k) { const char* a = lds + (s & 1) * 28672 + (t * 7 + k) * 2048 + lane * 16; Ut[t][k] = mk16(*(LAS u32x4*)a, *(LAS u32x4*)(a + 1024)); }
#pragma unroll
        for (int t = 0; t < 2; ++t)
#pragma unroll
            for (int k = 0; k < 7; ++k) asm volatile("" : "+v"(Ut[t][k]));
        if (s > 0) {
#pragma clang loop unroll(disable)
            for (unsigned spins = 0; spins < (1u << 18); ++spins) {
                asm volatile("" ::: "memory");
                const unsigned v = __builtin_amdgcn_raw_buffer_load_b32(rh, hb + OFF_MEM + (h + 6) * 256, 0, AUX_SC1);
                if (!__any(v == SENT)) break;
                if (HELP_SLEEP) __builtin_amdgcn_s_sleep(HELP_SLEEP);
            }
        }
        u32x4 R[7];
#pragma unroll
        for (int k = 0; k < 7; ++k) R[k] = ld16c(rh, hb + OFF_MEM + k * 1024 + lane * 16);
        unsigned mxv = 0;
#pragma unroll
        for (int k = 0; k < 7; ++k) mxv = max(mxv, mx4(R[k]));
        if (__builtin_expect(__any(mxv == SENT), 0)) {
#pragma clang loop unroll(disable)
            for (unsigned spins = 0; spins < (1u << 18); ++spins) {
                asm volatile("" ::: "memory");
                mxv = 0;
#pragma unroll
                for (int k = 0; k < 7; ++k) { R[k] = ld16c(rh, hb + OFF_MEM + k * 1024 + lane * 16); mxv = max(mxv, mx4(R[k])); }
                if (!__any(mxv == SENT)) break;
            }
        }
        f32x4 acc[2] = {{0, 0, 0, 0}, {0, 0, 0, 0}};
        asm volatile("" : "+v"(acc[0]), "+v"(acc[1]));
#pragma unroll
        for (int k = 0; k < 7; ++k)
#pragma unroll
            for (int t = 0; t < 2; ++t) acc[t] = SMF(asfrag(R[k]), Ut[t][k], acc[t], idxM);
        const float a0 = acc[0][0] + acc[0][1], b0 = acc[0][2] + acc[0][3], a1 = acc[1][0] + acc[1][1], b1 = acc[1][2] + acc[1][3];
        float ma = fmaxf(a0, a1), mb = fmaxf(b0, b1);
        ma = fmaxf(ma, dppf<0x128>(ma)); mb = fmaxf(mb, dppf<0x128>(mb));
        ma = fmaxf(ma, dppf<0x124>(ma)); mb = fmaxf(mb, dppf<0x124>(mb));
        ma = fmaxf(ma, dppf<0x122>(ma)); mb = fmaxf(mb, dppf<0x122>(mb));
        ma = fmaxf(ma, dppf<0x121>(ma)); mb = fmaxf(mb, dppf<0x121>(mb));
        const float ea0 = __builtin_amdgcn_exp2f(a0 - ma), ea1 = __builtin_amdgcn_exp2f(a1 - ma), eb0 = __builtin_amdgcn_exp2f(b0 - mb), eb1 = __builtin_amdgcn_exp2f(b1 - mb);
        const bool odd = n & 1;
        const float y0 = dppf<0xB1>(odd ? ea0 : eb0), y1 = dppf<0xB1>(odd ? ea1 : eb1);
        const unsigned d0 = cvtpk(odd ? y0 : ea0, odd ? eb0 : y0), d1 = cvtpk(odd ? y1 : ea1, odd ? eb1 : y1);
        const unsigned eo = hb + OFF_ET + h * 512 + (2 * g + (n & 1)) * 32 + (n >> 1) * 4;
        st4c(rh, eo, nosent(d0)); st4c(rh, eo + 256, nosent(d1));
        float la = ea0 + ea1, lb = eb0 + eb1;
        la += dppf<0x128>(la); lb += dppf<0x128>(lb);
        la += dppf<0x124>(la); lb += dppf<0x124>(lb);
        la += dppf<0x122>(la); lb += dppf<0x122>(lb);
        la += dppf<0x121>(la); lb += dppf<0x121>(lb);
        if (n < 4) st8c(rh, hb + OFF_ML + h * 128 + ((n >> 1) * 8 + 2 * g + (n & 1)) * 8, (u32x2){nosent(__float_as_uint(odd ? mb : ma)), nosent(__float_as_uint(odd ? lb : la))});
    }
}

DI void chain_role(int role, char* lds) {
    const int lane = threadIdx.x & 63, w = __builtin_amdgcn_readfirstlane(threadIdx.x >> 6), n = lane & 15, g = lane >> 4;
    const int h = role >> 1, beta = role & 1, ct = role < 28 ? role : 27;
    unsigned c[CH_NCONST];
    c[0] = lane * 16; c[1] = (n & 1) ? 0xEEEE : 0x4444; c[2] = (n >> 3) ? 0xEEEE : 0x4444;
    c[3] = lane * 16 + w * 1024; c[4] = lane * 16 + (w < 3 ? w + 4 : 0) * 1024; c[5] = CH_L_MEM + lane * 16;
    c[6] = CH_L_UT + (48 * w + n) * 16 + g * 4;
    c[7] = CH_L_UT + (192 + 16 * w + n) * 16 + g * 4;
    c[8] = CH_L_UT + (16 * g + 4 * ((lane & 3) >> 1) + ((lane >> 2) & 1) + 8 * ((lane >> 3) & 1)) * 16 + (lane & 1) * 8;
    c[9] = CH_L_MEM + ((14 * beta + (g >> 1)) * 16 + 2 * (n & 7) + (g & 1)) * 16;
    c[10] = OFF_SC + role * 128 + (n & 7) * 16 + (2 * (g & 1) + (g >> 1)) * 4;
    c[11] = OFF_ET + (4 * w + (n >> 3)) * 512 + (g >> 1) * 256 + (n & 7) * 32 + (g & 1) * 16;
    c[12] = OFF_ML + (4 * w + g) * 128 + (n & 7) * 8;
    c[13] = OFF_SC + ((4 * w + g) * 2 + (n >> 3)) * 128 + (n & 7) * 16;
    c[14] = CH_L_P + w * 512 + n * 64 + g * 16;
    { const int rr = lane >> 2, i = rr >> 1, par = rr & 1, t = lane & 3; c[15] = CH_L_P + i * 64 + (4 * t + 2 * par) * 4; }
    c[16] = OFF_MEM + ct * 256 + lane * 4;
    c[17] = CH_L_WB + w * CH_NLW * 2048 + lane * 16;
    { const int q = (lane >> 2) & 3, pp = lane & 3; c[18] = CH_L_UT + (64 * w + 8 * (2 * g + (pp >> 1)) + q) * 16 + (pp & 1) * 8; }
    { const int j = lane & 7, chk = lane >> 3; c[19] = (16 * beta + 4 * w + (chk >> 1)) * 1024 + ((chk & 1) * 32 + (h & 3) * 8 + j) * 16; }
    c[20] = (g >> 1) * FR + ((g & 1) * 32 + pi32(n)) * 16;
    c[21] = (g >> 1) * FR + ((g & 1) * 32 + pi32(16 + n)) * 16;
    c[22] = (g >> 1) * FR + ((g & 1) * 32 + 16 * (ct & 1) + n) * 16;
    { const int li = (role & 15) * 10 + min(lane, 9); c[23] = li < 64 ? li * 128 : li < 96 ? OFF_SC + (li - 64) * 128 : OFF_ET + (li - 96) * 128; }
    c[24] = OFF_FLG + ct * 128;
#pragma unroll
    for (int k = 0; k < CH_NCONST; ++k) *(LAS unsigned*)(lds + CH_L_WB + w * CH_NLW * 2048 + k * 256 + lane * 4) = c[k];
    const ChainBlock* b = (const ChainBlock*)(kparams()->ws + WS_BLK) + role;
    asm volatile("s_waitcnt lgkmcnt(0)\n" CHAIN_ASM_TEXT :: "s"(b), "s"(w) : CHAIN_ASM_CLOBBERS);
}

constexpr int LDS_BYTES = CH_L_END > L_WORK_END ? CH_L_END : L_WORK_END;

__global__ void __launch_bounds__(256, 1) main_kernel(Params p) {
    extern __shared__ __attribute__((aligned(16))) char lds[];
    const int b = blockIdx.x, x = b & 7, j = b >> 3;
    TL(b, 0);
    if (x < 2) {
        if (j < 16) chain_role(x * 16 + j, lds);
        else if (j < 24) helper_role(x * 8 + j - 16, lds);
        else aux_role(x * 8 + j - 24, lds);
        return;
    }
    auto bid = []() { int v = blockIdx.x; asm volatile("" : "+s"(v)); return v; };
    {
        const int bb = bid(), xg = (bb & 7) - 2, jj = bb >> 3, np = xg < 2 ? 6 : 5, p0 = xg < 2 ? 6 * xg : 12 + 5 * (xg - 2);
        if (jj < 5 * np) { const int pr = p0 + jj / 5; if (pr & 1) fast_task<1>(pr >> 1, jj % 5, lds); else fast_task<0>(pr >> 1, jj % 5, lds); }
        else {
            const int pr = xg < 2 ? 2 * xg + jj - 30 : 4 + 7 * (xg - 2) + jj - 25;
            if (pr & 1) proj_task<1>(1, pr >> 1, 2, lds); else proj_task<0>(0, pr >> 1, 2, lds);
        }
    }
    TL(b, 9);
    for (int i = j;; i += 32) {
        const int bb = bid(), xg = (bb & 7) - 2, np = xg < 2 ? 6 : 5, p0 = xg < 2 ? 6 * xg : 12 + 5 * (xg - 2);
        const int e0 = (p0 + 1) >> 1, ne = ((p0 + np + 1) >> 1) - e0;
        if (i >= 32 && i < 64) {
            const int r = (xg * 32 + (bb >> 3) + 128) % 192;
            if (r < 48) { proj_task<2>(2 + r % 3, 0, r / 3, lds); TL(bb, 24); }
        }
        if (i >= 13 * np + 2 * ne) break;
        const bool extra = i >= 13 * np;
        const int pr = extra ? 2 * (e0 + (i - 13 * np) % ne) : p0 + i % np, tile = extra ? (i - 13 * np) / ne : 3 + i / np;
        if (pr & 1) proj_task<1>(1, pr >> 1, tile, lds, i < 32); else proj_task<0>(0, pr >> 1, tile, lds, i < 32);
        TL(bb, 1 + i / 32);
    }
    {
        const int bb = bid(), wid = ((bb & 7) - 2) * 32 + (bb >> 3), r = (wid + 128) % 192;
        if (r >= 48 && r < 48 + 64) { kv_task((r - 48) >> 2, (r - 48) & 3, lds); TL(bb, 25); }
        else if (r >= 112 && r < 128) { outa_task(r - 112, lds); TL(bb, 26); }
    }
    TL(b, 5);
    for (int i = ((bid() & 7) - 2) * 32 + (bid() >> 3); i < NSEG * 7; i += 192) { tail_task(i / 7, i % 7, lds); TL(blockIdx.x, 6 + i / 192); }
}

extern "C" void kernel_launch(void* const* d_in, const int* in_sizes, int n_in, void* d_out, int out_size, void* d_ws, size_t ws_size, hipStream_t stream) {
    Params p;
    p.x = (const u16*)d_in[0]; p.wqkv = (const u16*)d_in[1]; p.wphi = (const u16*)d_in[2]; p.wo = (const u16*)d_in[3];
    p.mem0 = (const u16*)d_in[5]; p.Wq = (const u16*)d_in[6]; p.Wk = (const u16*)d_in[7]; p.Wv = (const u16*)d_in[8]; p.Wo = (const u16*)d_in[9];
    p.gw = (const u16*)d_in[11];
    p.out = (u16*)d_out;
    p.ws = (char*)(((size_t)d_ws + 2 * MB - 1) & ~(2 * MB - 1));
    (void)hipFuncSetAttribute((const void*)prep_kernel, hipFuncAttributeMaxDynamicSharedMemorySize, PL_END);
    (void)hipFuncSetAttribute((const void*)main_kernel, hipFuncAttributeMaxDynamicSharedMemorySize, LDS_BYTES);
    prep_kernel<<<256, 256, PL_END, stream>>>(p);
    main_kernel<<<256, 256, LDS_BYTES, stream>>>(p);
}
```
